```python
import torch

def _softmax(x, axis=-1):
    return torch.softmax(x, dim=axis)
def _gelu(x):
    return torch.nn.functional.gelu(x, approximate="tanh")
def _silu(x):
    return torch.nn.functional.silu(x)
def _sigmoid(x):
    return torch.sigmoid(x)
def _layernorm(x, axis=-1, eps=1e-5):
    m = x.mean(dim=axis, keepdim=True)
    v = x.var(dim=axis, keepdim=True, unbiased=False)
    return (x - m) / torch.sqrt(v + eps)
def _scatter_add(tgt, idx, src, axis=0):
    return tgt.clone().index_add_(axis, idx, src)
def _segment_sum(src, seg, n):
    out = torch.zeros((n,) + src.shape[1:], dtype=src.dtype)
    return out.index_add_(0, seg, src)
def _pad(x, pw):
    flat = tuple(v for pair in reversed(pw) for v in pair)
    return torch.nn.functional.pad(x, flat)

kernel_name = 'interleave_layers__ssm_conv_s4d__glu_gated_swiglu__prompt'

def setup_inputs():

    _w = (lambda a: (lambda t: t.to(torch.bfloat16) if t.is_floating_point() else t)(torch.as_tensor(a)))
    def _A_setup():
        Bsz, L, D, N = 1, 2048, 512, 128
        return {
            "u": _w((lambda *s: torch.randn(*s, dtype=torch.bfloat16) * 0.05)(Bsz, L, D)),
            "log_A_real": _w((lambda *s: torch.randn(*s, dtype=torch.bfloat16) * 3.2)(D, N) * 0.5),
            "A_imag": _w((lambda *s: torch.randn(*s, dtype=torch.bfloat16) * 0.05)(D, N) * 3.14),
            "C_real": _w((lambda *s: torch.randn(*s, dtype=torch.bfloat16) * 0.05)(D, N) / N ** 0.5),
            "C_imag": _w((lambda *s: torch.randn(*s, dtype=torch.bfloat16) * 0.05)(D, N) / N ** 0.5),
            "log_dt": _w((lambda *s: torch.randn(*s, dtype=torch.bfloat16) * 0.05)(D) * 0.5 - 4.0),
            "D_skip": _w((lambda *s: torch.randn(*s, dtype=torch.bfloat16) * 0.05)(D)),
        }
    _A_in = _A_setup()

    def _B_setup():
        B, S, D, F = 1, 2048, 512, 1376
        return {
            "x": _w((lambda *s: torch.randn(*s, dtype=torch.bfloat16) * 0.05)(B, S, D)),
            "w1": _w((lambda *s: torch.randn(*s, dtype=torch.bfloat16) * 0.4)(D, F) * (2.0 / D) ** 0.5),
            "w3": _w((lambda *s: torch.randn(*s, dtype=torch.bfloat16) * 0.4)(D, F) * (2.0 / D) ** 0.5),
            "w2": _w((lambda *s: torch.randn(*s, dtype=torch.bfloat16) * 0.4)(F, D) * (2.0 / F) ** 0.5),
        }
    _B_in = _B_setup()

    return {
        "A_u": _A_in["u"],
        "A_log_A_real": _A_in["log_A_real"],
        "A_A_imag": _A_in["A_imag"],
        "A_C_real": _A_in["C_real"],
        "A_C_imag": _A_in["C_imag"],
        "A_log_dt": _A_in["log_dt"],
        "A_D_skip": _A_in["D_skip"],
        "B_x": _B_in["x"],
        "B_w1": _B_in["w1"],
        "B_w3": _B_in["w3"],
        "B_w2": _B_in["w2"],
    }

def reference(A_u, A_log_A_real, A_A_imag, A_C_real, A_C_imag, A_log_dt, A_D_skip, B_x, B_w1, B_w3, B_w2):
    def _A_ref(u, log_A_real, A_imag, C_real, C_imag, log_dt, D_skip):
        L = u.shape[1]
        dt = torch.exp(log_dt)
        A = -torch.exp(log_A_real) + 1j * A_imag
        C = (C_real + 1j * C_imag) * dt[:, None]
        pos = (lambda x, dt: x.to(dt))(torch.arange(L), log_dt.dtype)
        P = torch.exp((A * dt[:, None])[:, :, None] * pos[None, None, :])
        K = 2.0 * torch.einsum("dn,dnl->dl", C, P).real
        L2 = 2 * L
        u_t = (lambda x, *d: x.permute(*d))(u, 0, 2, 1)
        Kf = (lambda x, *a, **k: torch.fft.rfft(x.float(), *a, **k))(K, L2)
        Uf = (lambda x, *a, **k: torch.fft.rfft(x.float(), *a, **k))(u_t, L2)
        y = (lambda x, *a, **k: torch.fft.irfft(x, *a, **k).to(torch.bfloat16))(Uf * Kf[None, :, :], L2)[:, :, :L]
        y = (lambda x, *d: x.permute(*d))(y, 0, 2, 1)
        return y + u * D_skip[None, None, :]

    def _B_ref(x, w1, w3, w2):
        gate = torch.einsum("bsd,df->bsf", x, w1)
        up = torch.einsum("bsd,df->bsf", x, w3)
        h = _silu(gate) * up
        out = torch.einsum("bsf,fd->bsd", h, w2)
        return out

    x = A_u
    x = x + _A_ref(_layernorm(x, axis=-1), A_log_A_real, A_A_imag, A_C_real, A_C_imag, A_log_dt, A_D_skip)
    x = x + _B_ref(_layernorm(x, axis=-1), B_w1, B_w3, B_w2)
    x = x + _A_ref(_layernorm(x, axis=-1), A_log_A_real, A_A_imag, A_C_real, A_C_imag, A_log_dt, A_D_skip)
    x = x + _B_ref(_layernorm(x, axis=-1), B_w1, B_w3, B_w2)
    return x
```

```cpp
#include <hip/hip_runtime.h>
#include <hip/hip_bf16.h>
#include <stdint.h>

typedef unsigned short u16;
typedef unsigned int u32;
typedef short s16x4 __attribute__((ext_vector_type(4)));
typedef u32 u32x2 __attribute__((ext_vector_type(2)));
typedef u32 u32x4 __attribute__((ext_vector_type(4)));
typedef _Float16 h16x2 __attribute__((ext_vector_type(2)));
typedef _Float16 h16x8 __attribute__((ext_vector_type(8)));
typedef __bf16 b16x2 __attribute__((ext_vector_type(2)));
typedef __bf16 b16x8 __attribute__((ext_vector_type(8)));
typedef float f32x2 __attribute__((ext_vector_type(2)));
typedef float f32x4 __attribute__((ext_vector_type(4)));

#define DEV __device__ __forceinline__
#define STAMP(kid, i)

constexpr int L_ = 2048, D_ = 512, N_ = 128, F_ = 1376;
constexpr int XP = 2176;
constexpr int KXP = 2088;
constexpr int NTILE = F_ / 8;
constexpr int KS3 = F_ / 32;

constexpr size_t MB = 1u << 20;
constexpr size_t WS_XT0 = 24 * MB, WS_XT1 = 27 * MB, WS_XT2 = 30 * MB, WS_XT3 = 33 * MB;
constexpr size_t WS_KX = 8 * MB;
constexpr size_t WS_KSC = 11 * MB;
constexpr size_t WS_ST1 = 11 * MB + 65536;
constexpr size_t WS_SP = 11 * MB + 2 * 65536;
constexpr size_t WS_WGU = 12 * MB;
constexpr size_t WS_WD = 16 * MB;
constexpr size_t WS_H = 18 * MB;

DEV float bf_lo(u32 v) { return __builtin_bit_cast(float, v << 16); }
DEV float bf_hi(u32 v) { return __builtin_bit_cast(float, v & 0xffff0000u); }
DEV float bf2f(u16 v) { return __builtin_bit_cast(float, (u32)v << 16); }
DEV u32 pk_bf(float a, float b) {
  f32x2 z = {a, b};
  return __builtin_bit_cast(u32, __builtin_convertvector(z, b16x2));
}
DEV float bfr(float a) { return bf_lo(pk_bf(a, a)); }
DEV u32 pk_h(float a, float b) {
  h16x2 z = {(_Float16)a, (_Float16)b};
  return __builtin_bit_cast(u32, z);
}
DEV float wave_sum(float v) {
#pragma unroll
  for (int o = 32; o > 0; o >>= 1) v += __shfl_xor(v, o);
  return v;
}
struct cplx { float r, i; };
DEV cplx cmul(cplx a, cplx b) { return {a.r * b.r - a.i * b.i, a.r * b.i + a.i * b.r}; }
DEV cplx cexp_(float zr, float zi, float p) {
  float m = expf(zr * p), s, c;
  sincosf(zi * p, &s, &c);
  return {m * c, m * s};
}
DEV void split2(float a, float b, u32& hi, u32& lo) {
  hi = pk_bf(a, b);
  lo = pk_bf(a - bf_lo(hi), b - bf_hi(hi));
}
DEV u32 perm_lo(u32 a, u32 b) { return __builtin_amdgcn_perm(b, a, 0x05040100u); }
DEV u32 perm_hi(u32 a, u32 b) { return __builtin_amdgcn_perm(b, a, 0x07060302u); }

constexpr int KG_ROW = 132;
constexpr int KG_CH = (48 * 2 + 16 * 2) * KG_ROW;
__global__ __launch_bounds__(512) void k0_prep(const u16* __restrict__ x, const u16* __restrict__ lar,
                                               const u16* __restrict__ aim, const u16* __restrict__ cre,
                                               const u16* __restrict__ cim, const u16* __restrict__ ldt,
                                               const u16* __restrict__ w1, const u16* __restrict__ w3,
                                               const u16* __restrict__ w2, char* __restrict__ ws) {
  __shared__ __attribute__((aligned(16))) u32 sm[2 * KG_CH + 2 * 48 * 16 + 8 * 256 + 16];
  u32* sA = sm;
  float* sK = (float*)(sm + 2 * KG_CH);
  u32* sT = sm + 2 * KG_CH + 2 * 48 * 16;
  float* sB = (float*)(sT + 8 * 256);
  const int b = blockIdx.x, tid = threadIdx.x, lane = tid & 63, w = tid >> 6;

  STAMP(0, 0);
  const int rg = (b & 7) * 32 + (b >> 3);
  const int trow = rg * 8 + w;
  const u32x4 xv = *(const u32x4*)(x + (size_t)trow * D_ + lane * 8);

  const int ch = tid >> 8, part = (tid >> 7) & 1, n = tid & 127;
  const int d = b * 2 + ch;
  const u16 p_lar = lar[d * N_ + n], p_aim = aim[d * N_ + n], p_cre = cre[d * N_ + n], p_cim = cim[d * N_ + n];
  const u16 p_ldt = ldt[d];

  {
    const int task = tid * 256 + b;
    if (tid < 43) {
      const int tk = b * 43 + tid;
      const int kb = tk / NTILE, tau = tk % NTILE;
      u32x4 a[8], c[8];
#pragma unroll
      for (int j = 0; j < 8; ++j) {
        a[j] = *(const u32x4*)(w1 + (size_t)(kb * 8 + j) * F_ + tau * 8);
        c[j] = *(const u32x4*)(w3 + (size_t)(kb * 8 + j) * F_ + tau * 8);
      }
      u32x4* dst = (u32x4*)(ws + WS_WGU) + ((size_t)(tau * 16 + (kb >> 2)) * 64 + (kb & 3) * 16);
#pragma unroll
      for (int q = 0; q < 4; ++q) {
        u32x4 o0 = {perm_lo(a[0][q], a[1][q]), perm_lo(a[2][q], a[3][q]), perm_lo(a[4][q], a[5][q]), perm_lo(a[6][q], a[7][q])};
        u32x4 o1 = {perm_hi(a[0][q], a[1][q]), perm_hi(a[2][q], a[3][q]), perm_hi(a[4][q], a[5][q]), perm_hi(a[6][q], a[7][q])};
        u32x4 o2 = {perm_lo(c[0][q], c[1][q]), perm_lo(c[2][q], c[3][q]), perm_lo(c[4][q], c[5][q]), perm_lo(c[6][q], c[7][q])};
        u32x4 o3 = {perm_hi(c[0][q], c[1][q]), perm_hi(c[2][q], c[3][q]), perm_hi(c[4][q], c[5][q]), perm_hi(c[6][q], c[7][q])};
        dst[4 * q + 0] = o0; dst[4 * q + 1] = o1; dst[4 * q + 2] = o2; dst[4 * q + 3] = o3;
      }
    } else if (tid >= 64 && tid < 64 + 43) {
      const int tk = b * 43 + (tid - 64);
      const int dc8 = tk & 63, kg = tk >> 6;
      const int ks = kg >> 2, g = kg & 3;
      u32x4 a[8];
#pragma unroll
      for (int j = 0; j < 8; ++j) {
        const int f = 32 * ks + 8 * (j >> 1) + 2 * g + (j & 1);
        a[j] = *(const u32x4*)(w2 + (size_t)f * D_ + dc8 * 8);
      }
      u32x4* dst = (u32x4*)(ws + WS_WD) + ((size_t)((dc8 >> 1) * KS3 + ks) * 64 + g * 16 + (dc8 & 1) * 8);
#pragma unroll
      for (int q = 0; q < 4; ++q) {
        u32x4 o0 = {perm_lo(a[0][q], a[1][q]), perm_lo(a[2][q], a[3][q]), perm_lo(a[4][q], a[5][q]), perm_lo(a[6][q], a[7][q])};
        u32x4 o1 = {perm_hi(a[0][q], a[1][q]), perm_hi(a[2][q], a[3][q]), perm_hi(a[4][q], a[5][q]), perm_hi(a[6][q], a[7][q])};
        dst[2 * q + 0] = o0; dst[2 * q + 1] = o1;
      }
    }
    (void)task;
  }

  STAMP(0, 1);
  {
    float v[8];
#pragma unroll
    for (int j = 0; j < 4; ++j) { v[2 * j] = bf_lo(xv[j]); v[2 * j + 1] = bf_hi(xv[j]); }
    float s = 0.f;
#pragma unroll
    for (int j = 0; j < 8; ++j) s += v[j];
    const float mean = wave_sum(s) * (1.f / D_);
    float q = 0.f;
#pragma unroll
    for (int j = 0; j < 8; ++j) { float e = v[j] - mean; q += e * e; }
    const float var = wave_sum(q) * (1.f / D_);
    if (lane == 0) {
      const float m = bfr(mean);
      const float sd = bfr(sqrtf(bfr(bfr(var) + 1e-5f)));
      ((f32x2*)(ws + WS_ST1))[trow] = f32x2{m, sd};
    }
    *(u32x4*)(sT + w * 256 + lane * 4) = xv;
  }

  STAMP(0, 2);
  {
    const float dt = bfr(expf(bf2f(p_ldt)));
    const float ar = -bfr(expf(bf2f(p_lar)));
    const float zr = ar * dt, zi = bf2f(p_aim) * dt;
    const cplx c0 = {bf2f(p_cre) * dt, bf2f(p_cim) * dt};
    u32* Ahi = sA + ch * KG_CH;
    u32* Alo = Ahi + 48 * KG_ROW;
    u32* Bhi = Alo + 48 * KG_ROW;
    u32* Blo = Bhi + 16 * KG_ROW;
    const cplx w1c = cexp_(zr, zi, 1.f);
    const cplx w128 = cexp_(zr, zi, 128.f);
    auto putA = [&](int xr, cplx g) {
      u32 hi, lo; split2(g.r, -g.i, hi, lo);
      Ahi[xr * KG_ROW + n] = hi; Alo[xr * KG_ROW + n] = lo;
    };
    if (part == 0) {
      const cplx w8 = cexp_(zr, zi, 8.f);
      cplx h = {1.f, 0.f};
#pragma unroll
      for (int y = 0; y < 16; ++y) {
        u32 hi, lo; split2(h.r, h.i, hi, lo);
        Bhi[y * KG_ROW + n] = hi; Blo[y * KG_ROW + n] = lo;
        h = cmul(h, w8);
      }
      cplx g0 = c0, g1 = cmul(c0, w128);
#pragma unroll
      for (int r = 0; r < 8; ++r) {
        putA(r, g0); putA(8 + r, g1);
        g0 = cmul(g0, w1c); g1 = cmul(g1, w1c);
      }
      float bnd = wave_sum(fabsf(c0.r) + fabsf(c0.i));
      if (lane == 0) sB[ch * 2 + (w & 1)] = bnd;
    } else {
      const cplx e2 = cmul(w1c, w1c), e4 = cmul(e2, e2), e6 = cmul(e4, e2);
      cplx g = cmul(c0, cexp_(zr, zi, 256.f));
#pragma unroll
      for (int c = 2; c < 4; ++c) {
        putA(16 + 4 * (c - 2) + 0, g);
        putA(16 + 4 * (c - 2) + 1, cmul(g, e2));
        putA(16 + 4 * (c - 2) + 2, cmul(g, e4));
        putA(16 + 4 * (c - 2) + 3, cmul(g, e6));
        g = cmul(g, w128);
      }
#pragma unroll
      for (int c = 4; c < 8; ++c) {
        putA(24 + 2 * (c - 4) + 0, g);
        putA(24 + 2 * (c - 4) + 1, cmul(g, e4));
        g = cmul(g, w128);
      }
      g = cmul(c0, cexp_(zr, zi, 1024.f));
#pragma unroll
      for (int c = 8; c <= 16; ++c) {
        putA(32 + (c - 8), g);
        g = cmul(g, w128);
      }
#pragma unroll
      for (int xr = 41; xr < 48; ++xr) { Ahi[xr * KG_ROW + n] = 0u; Alo[xr * KG_ROW + n] = 0u; }
    }
  }
  __syncthreads();

  STAMP(0, 3);
  {
    const u16* sTh = (const u16*)sT;
    u32x4 o;
#pragma unroll
    for (int j = 0; j < 4; ++j)
      o[j] = (u32)sTh[(2 * j) * 512 + tid] | ((u32)sTh[(2 * j + 1) * 512 + tid] << 16);
    *(u32x4*)((u16*)(ws + WS_XT0) + (size_t)tid * XP + rg * 8) = o;
  }

  if (w < 6) {
    const int c2 = w / 3, rt = w % 3;
    const u32* Ahi = sA + c2 * KG_CH;
    const u32* Alo = Ahi + 48 * KG_ROW;
    const u32* Bhi = Alo + 48 * KG_ROW;
    const u32* Blo = Bhi + 16 * KG_ROW;
    const int r16 = lane & 15, g = lane >> 4;
    f32x4 acc = {0.f, 0.f, 0.f, 0.f};
#pragma unroll
    for (int ks = 0; ks < 8; ++ks) {
      const int ao = (rt * 16 + r16) * KG_ROW + ks * 16 + g * 4;
      const int bo = r16 * KG_ROW + ks * 16 + g * 4;
      const b16x8 ah = __builtin_bit_cast(b16x8, *(const u32x4*)(Ahi + ao));
      const b16x8 al = __builtin_bit_cast(b16x8, *(const u32x4*)(Alo + ao));
      const b16x8 bh = __builtin_bit_cast(b16x8, *(const u32x4*)(Bhi + bo));
      const b16x8 bl = __builtin_bit_cast(b16x8, *(const u32x4*)(Blo + bo));
      acc = __builtin_amdgcn_mfma_f32_16x16x32_bf16(al, bh, acc, 0, 0, 0);
      acc = __builtin_amdgcn_mfma_f32_16x16x32_bf16(ah, bl, acc, 0, 0, 0);
      acc = __builtin_amdgcn_mfma_f32_16x16x32_bf16(ah, bh, acc, 0, 0, 0);
    }
#pragma unroll
    for (int r = 0; r < 4; ++r) sK[(c2 * 48 + rt * 16 + g * 4 + r) * 16 + r16] = acc[r];
  }
  __syncthreads();

  STAMP(0, 4);
  {
    const int i = tid & 255;
    const float bound = 2.f * (sB[ch * 2] + sB[ch * 2 + 1]);
    int e;
    (void)frexpf(bound, &e);
    e = min(max(e, -100), 100);
    const float scale = ldexpf(2.f, 15 - e);
    float v[8];
#pragma unroll
    for (int j = 0; j < 8; ++j) {
      const int l = 2047 - (8 * i + j);
      const int p = (int)bfr((float)l);
      const int y = (p >> 3) & 15, c = p >> 7, r = p & 7;
      const int xr = c < 2 ? 8 * c + r : c < 4 ? 16 + 4 * (c - 2) + (r >> 1) : c < 8 ? 24 + 2 * (c - 4) + (r >> 2) : 32 + (c - 8);
      v[j] = sK[(ch * 48 + xr) * 16 + y] * scale;
    }
    u16* kx = (u16*)(ws + WS_KX) + (size_t)d * KXP;
    *(u32x4*)(kx + 8 * i) = u32x4{pk_h(v[0], v[1]), pk_h(v[2], v[3]), pk_h(v[4], v[5]), pk_h(v[6], v[7])};
    if (i < 5) *(u32x4*)(kx + 2048 + 8 * i) = u32x4{0u, 0u, 0u, 0u};
    if (i == 0) ((float*)(ws + WS_KSC))[d] = ldexpf(1.f, e - 15 - 10);
  }
  STAMP(0, 5);
}

constexpr int KC_PITCH = 4160;
constexpr int CV_KC = 0, CV_U = 2 * 4 * KC_PITCH, CV_UP = (256 + 2048) * 2, CV_Y = CV_U + 2 * CV_UP;
constexpr int CV_LDS = CV_Y + 4 * 8192;
template <bool PARTIAL>
__global__ __launch_bounds__(256) void k_conv(const u16* __restrict__ xT, u16* __restrict__ xTo,
                                              const char* __restrict__ ws, const u16* __restrict__ dskip) {
  __shared__ __attribute__((aligned(16))) char sm[CV_LDS];
  const int b = blockIdx.x, tid = threadIdx.x, lane = tid & 63, w = tid >> 6;
  const int d0 = b * 2;
  STAMP(PARTIAL ? 4 : 1, 0);
  const u32x4 x0 = *(const u32x4*)(xT + (size_t)d0 * XP + tid * 8);
  const u32x4 x1 = *(const u32x4*)(xT + (size_t)(d0 + 1) * XP + tid * 8);
  const int kch = tid >> 7, ki = tid & 127;
  const u16* kx = (const u16*)(ws + WS_KX) + (size_t)(d0 + kch) * KXP;
  u32x4 kw[3], kw2[3];
#pragma unroll
  for (int j = 0; j < 3; ++j) kw[j] = *(const u32x4*)(kx + 16 * ki + 8 * j);
  if (ki < 2) {
#pragma unroll
    for (int j = 0; j < 3; ++j) kw2[j] = *(const u32x4*)(kx + 16 * (ki + 128) + 8 * j);
  }
  float mv[8], sv[8];
  if (!PARTIAL) {
    const f32x4* st = (const f32x4*)(ws + WS_ST1) + tid * 4;
#pragma unroll
    for (int j = 0; j < 4; ++j) {
      const f32x4 s4 = st[j];
      mv[2 * j] = s4[0]; sv[2 * j] = s4[1]; mv[2 * j + 1] = s4[2]; sv[2 * j + 1] = s4[3];
    }
  } else {
    f32x4 sp[8][4];
#pragma unroll
    for (int c = 0; c < 8; ++c)
#pragma unroll
      for (int j = 0; j < 4; ++j) sp[c][j] = ((const f32x4*)(ws + WS_SP))[(size_t)c * 1024 + tid * 4 + j];
#pragma unroll
    for (int j = 0; j < 8; ++j) {
      float mean = 0.f;
#pragma unroll
      for (int c = 0; c < 8; ++c) mean += sp[c][j >> 1][(j & 1) * 2];
      mean *= 0.125f;
      float m2 = 0.f, dv = 0.f;
#pragma unroll
      for (int c = 0; c < 8; ++c) {
        const float e = sp[c][j >> 1][(j & 1) * 2] - mean;
        m2 += sp[c][j >> 1][(j & 1) * 2 + 1];
        dv += e * e;
      }
      const float var = (m2 + 64.f * dv) * (1.f / D_);
      mv[j] = bfr(mean);
      sv[j] = bfr(sqrtf(bfr(bfr(var) + 1e-5f)));
    }
  }
  const float ds0 = bf2f(dskip[d0]), ds1 = bf2f(dskip[d0 + 1]);
  const float ksc0 = ((const float*)(ws + WS_KSC))[d0], ksc1 = ((const float*)(ws + WS_KSC))[d0 + 1];

  STAMP(PARTIAL ? 4 : 1, 1);
  {
    auto put = [&](const u32x4* q, int i) {
      u32 wd[12];
#pragma unroll
      for (int j = 0; j < 12; ++j) wd[j] = q[j >> 2][j & 3];
      char* base = sm + CV_KC + kch * 4 * KC_PITCH + i * 32;
#pragma unroll
      for (int hf = 0; hf < 2; ++hf) {
        const int o = hf * 4;
        *(u32x4*)(base + 0 * KC_PITCH + hf * 16) = u32x4{wd[o], wd[o + 1], wd[o + 2], wd[o + 3]};
        *(u32x4*)(base + 1 * KC_PITCH + hf * 16) =
            u32x4{__builtin_amdgcn_alignbit(wd[o + 1], wd[o], 16), __builtin_amdgcn_alignbit(wd[o + 2], wd[o + 1], 16),
                  __builtin_amdgcn_alignbit(wd[o + 3], wd[o + 2], 16), __builtin_amdgcn_alignbit(wd[o + 4], wd[o + 3], 16)};
        *(u32x4*)(base + 2 * KC_PITCH + hf * 16) = u32x4{wd[o + 1], wd[o + 2], wd[o + 3], wd[o + 4]};
        *(u32x4*)(base + 3 * KC_PITCH + hf * 16) =
            u32x4{__builtin_amdgcn_alignbit(wd[o + 2], wd[o + 1], 16), __builtin_amdgcn_alignbit(wd[o + 3], wd[o + 2], 16),
                  __builtin_amdgcn_alignbit(wd[o + 4], wd[o + 3], 16), __builtin_amdgcn_alignbit(wd[o + 5], wd[o + 4], 16)};
      }
    };
    put(kw, ki);
    if (ki < 2) put(kw2, ki + 128);
  }
  float z0[8], z1[8];
  {
    u32 uh0[4], uh1[4];
    float u0[8], u1[8];
#pragma unroll
    for (int j = 0; j < 8; ++j) {
      const float a = (j & 1) ? bf_hi(x0[j >> 1]) : bf_lo(x0[j >> 1]);
      const float c = (j & 1) ? bf_hi(x1[j >> 1]) : bf_lo(x1[j >> 1]);
      u0[j] = bfr(bfr(a - mv[j]) / sv[j]);
      u1[j] = bfr(bfr(c - mv[j]) / sv[j]);
      z0[j] = bfr(u0[j] * ds0);
      z1[j] = bfr(u1[j] * ds1);
    }
#pragma unroll
    for (int j = 0; j < 4; ++j) {
      uh0[j] = pk_h(u0[2 * j] * 1024.f, u0[2 * j + 1] * 1024.f);
      uh1[j] = pk_h(u1[2 * j] * 1024.f, u1[2 * j + 1] * 1024.f);
    }
    *(u32x4*)(sm + CV_U + 512 + tid * 16) = u32x4{uh0[0], uh0[1], uh0[2], uh0[3]};
    *(u32x4*)(sm + CV_U + CV_UP + 512 + tid * 16) = u32x4{uh1[0], uh1[1], uh1[2], uh1[3]};
    if (tid < 64) *(u32x4*)(sm + CV_U + (tid >> 5) * CV_UP + (tid & 31) * 16) = u32x4{0u, 0u, 0u, 0u};
  }
  __syncthreads();

  STAMP(PARTIAL ? 4 : 1, 2);
  {
    const int ch = w >> 1, half = w & 1;
    const int a = lane & 15, g = lane >> 4;
    const int phi = (3 - a) & 3;
    const char* abase = sm + CV_KC + ch * 4 * KC_PITCH + phi * KC_PITCH + 2 * (2055 - a - 8 * g - phi);
    const char* bbase = sm + CV_U + ch * CV_UP + 2 * (256 + 16 * a + 8 - 8 * g);
    f32x4 acc[8];
#pragma unroll
    for (int c = 0; c < 8; ++c) acc[c] = f32x4{0.f, 0.f, 0.f, 0.f};
#pragma unroll
    for (int rr = 0; rr < 4; ++rr) {
      const int r = half * 4 + rr;
      h16x8 A[8], B[8];
#pragma unroll
      for (int s = 0; s < 8; ++s) {
        const char* p = abase - 64 * (r + 8 * s);
        const u32x2 lo = *(const u32x2*)p, hi = *(const u32x2*)(p + 8);
        A[s] = __builtin_bit_cast(h16x8, u32x4{lo[0], lo[1], hi[0], hi[1]});
        B[s] = __builtin_bit_cast(h16x8, *(const u32x4*)(bbase + 512 * s - 64 * r));
      }
#pragma unroll
      for (int c = 0; c < 8; ++c)
#pragma unroll
        for (int s = 0; s <= c; ++s)
          acc[c] = __builtin_amdgcn_mfma_f32_16x16x32_f16(A[s], B[c - s], acc[c], 0, 0, 0);
    }
#pragma unroll
    for (int c = 0; c < 8; ++c) *(f32x4*)(sm + CV_Y + w * 8192 + c * 1024 + a * 64 + g * 16) = acc[c];
  }
  __syncthreads();

  STAMP(PARTIAL ? 4 : 1, 3);
  {
    const f32x4* yp = (const f32x4*)(sm + CV_Y) + tid * 2;
    const f32x4 a0 = yp[0] + yp[512], a1 = yp[1] + yp[513];
    const f32x4 c0 = yp[1024] + yp[1536], c1 = yp[1025] + yp[1537];
    float o0[8], o1[8];
#pragma unroll
    for (int j = 0; j < 8; ++j) {
      const float ya = (j < 4 ? a0[j & 3] : a1[j & 3]) * ksc0;
      const float yc = (j < 4 ? c0[j & 3] : c1[j & 3]) * ksc1;
      const float xa = (j & 1) ? bf_hi(x0[j >> 1]) : bf_lo(x0[j >> 1]);
      const float xc = (j & 1) ? bf_hi(x1[j >> 1]) : bf_lo(x1[j >> 1]);
      o0[j] = xa + bfr(bfr(ya) + z0[j]);
      o1[j] = xc + bfr(bfr(yc) + z1[j]);
    }
    *(u32x4*)(xTo + (size_t)d0 * XP + tid * 8) =
        u32x4{pk_bf(o0[0], o0[1]), pk_bf(o0[2], o0[3]), pk_bf(o0[4], o0[5]), pk_bf(o0[6], o0[7])};
    *(u32x4*)(xTo + (size_t)(d0 + 1) * XP + tid * 8) =
        u32x4{pk_bf(o1[0], o1[1]), pk_bf(o1[2], o1[3]), pk_bf(o1[4], o1[5]), pk_bf(o1[6], o1[7])};
  }
  STAMP(PARTIAL ? 4 : 1, 4);
}

constexpr int GU_X = 0, GU_RED = 512 * 192, GU_LDS = GU_RED + 8 * 96 * 8;
__global__ __launch_bounds__(512) void k_gu(const u16* __restrict__ xT, char* __restrict__ ws) {
  __shared__ __attribute__((aligned(16))) char sm[GU_LDS];
  const int tid = threadIdx.x, lane = tid & 63, w = __builtin_amdgcn_readfirstlane(tid >> 6);
  const int b = blockIdx.x;
  const int xcd = b & 7, lin = (xcd < 2 ? xcd * 31 : 62 + (xcd - 2) * 30) + (b >> 3);
  const int hh = lin / 121, rem = lin % 121;
  const int fg = rem / 11, rt = hh * 11 + rem % 11;
  const int t0 = min(rt * 96, L_ - 96);
  const int tau0 = fg * 16 + 2 * w;
  const bool active = tau0 < NTILE;

  STAMP(2, 0);
  u32x4 xr[12];
#pragma unroll
  for (int i = 0; i < 12; ++i) {
    const int ci = tid + 512 * i, dd = ci / 12, c = ci % 12;
    xr[i] = *(const u32x4*)(xT + (size_t)dd * XP + t0 + 8 * c);
  }
  u32x4 wf[2][16];
  {
    const u32x4* wp = (const u32x4*)(ws + WS_WGU) + (size_t)(active ? tau0 : 0) * 1024 + lane;
#pragma unroll
    for (int ks = 0; ks < 16; ++ks) {
      wf[0][ks] = wp[ks * 64];
      wf[1][ks] = wp[1024 + ks * 64];
    }
  }
#pragma unroll
  for (int i = 0; i < 12; ++i) {
    const int ci = tid + 512 * i, dd = ci / 12, c = ci % 12;
    *(u32x4*)(sm + GU_X + 192 * dd + 16 * (c ^ (((dd >> 3) & 1) << 1))) = xr[i];
  }
  __syncthreads();
  STAMP(2, 1);

  const int g = lane >> 4, q = (lane & 15) >> 2, p = lane & 3, r16 = lane & 15;
  const int sw = (g & 1) * 32;
  const char* xbE = sm + GU_X + 192 * (8 * g + q) + 8 * p + sw;
  const char* xbO = sm + GU_X + 192 * (8 * g + q) + 8 * p - sw;
  float piv[6], s1[6], s2[6];
#pragma unroll
  for (int tt = 0; tt < 6; ++tt) {
    piv[tt] = bf2f(*(const u16*)(sm + GU_X + (16 * tt + r16) * 2));
    s1[tt] = 0.f; s2[tt] = 0.f;
  }
  f32x4 acc[2][6];
#pragma unroll
  for (int tt = 0; tt < 6; ++tt) { acc[0][tt] = f32x4{0.f, 0.f, 0.f, 0.f}; acc[1][tt] = f32x4{0.f, 0.f, 0.f, 0.f}; }
  float wsum[2] = {0.f, 0.f};
#pragma unroll
  for (int ks = 0; ks < 16; ++ks) {
    u32x4 xf[6];
#pragma unroll
    for (int tt = 0; tt < 6; ++tt) {
      const char* pa = ((tt & 1) ? xbO : xbE) + 6144 * ks + 32 * tt;
      const s16x4 lo = __builtin_amdgcn_ds_read_tr16_b64_v4i16((__attribute__((address_space(3))) s16x4*)(pa));
      const s16x4 hi = __builtin_amdgcn_ds_read_tr16_b64_v4i16((__attribute__((address_space(3))) s16x4*)(pa + 768));
      const u32x2 l2 = __builtin_bit_cast(u32x2, lo), h2 = __builtin_bit_cast(u32x2, hi);
      xf[tt] = u32x4{l2[0], l2[1], h2[0], h2[1]};
    }
#pragma unroll
    for (int tt = 0; tt < 6; ++tt) {
      acc[0][tt] = __builtin_amdgcn_mfma_f32_16x16x32_bf16(__builtin_bit_cast(b16x8, wf[0][ks]), __builtin_bit_cast(b16x8, xf[tt]), acc[0][tt], 0, 0, 0);
      acc[1][tt] = __builtin_amdgcn_mfma_f32_16x16x32_bf16(__builtin_bit_cast(b16x8, wf[1][ks]), __builtin_bit_cast(b16x8, xf[tt]), acc[1][tt], 0, 0, 0);
    }
#pragma unroll
    for (int tl = 0; tl < 2; ++tl)
#pragma unroll
      for (int j = 0; j < 4; ++j) wsum[tl] += bf_lo(wf[tl][ks][j]) + bf_hi(wf[tl][ks][j]);
    if ((ks >> 1) == w) {
#pragma unroll
      for (int tt = 0; tt < 6; ++tt)
#pragma unroll
        for (int j = 0; j < 4; ++j) {
          const float e0 = bf_lo(xf[tt][j]) - piv[tt], e1 = bf_hi(xf[tt][j]) - piv[tt];
          s1[tt] += e0 + e1;
          s2[tt] += e0 * e0 + e1 * e1;
        }
    }
  }
  STAMP(2, 2);
#pragma unroll
  for (int tt = 0; tt < 6; ++tt) {
    s1[tt] += __shfl_xor(s1[tt], 16); s1[tt] += __shfl_xor(s1[tt], 32);
    s2[tt] += __shfl_xor(s2[tt], 16); s2[tt] += __shfl_xor(s2[tt], 32);
    if (g == 0) *(f32x2*)(sm + GU_RED + (w * 96 + tt * 16 + r16) * 8) = f32x2{s1[tt], s2[tt]};
  }
  float S[2][4];
#pragma unroll
  for (int tl = 0; tl < 2; ++tl) {
    wsum[tl] += __shfl_xor(wsum[tl], 16); wsum[tl] += __shfl_xor(wsum[tl], 32);
#pragma unroll
    for (int r = 0; r < 4; ++r) S[tl][r] = __shfl(wsum[tl], 4 * g + r);
  }
  __syncthreads();
  STAMP(2, 3);
  if (!active) return;
  u16* hb = (u16*)(ws + WS_H);
#pragma unroll
  for (int tt = 0; tt < 6; ++tt) {
    float a1 = 0.f, a2 = 0.f;
#pragma unroll
    for (int ww = 0; ww < 8; ++ww) {
      const f32x2 v = *(const f32x2*)(sm + GU_RED + (ww * 96 + tt * 16 + r16) * 8);
      a1 += v[0]; a2 += v[1];
    }
    a1 *= (1.f / D_); a2 *= (1.f / D_);
    const float m = bfr(piv[tt] + a1);
    const float var = fmaxf(a2 - a1 * a1, 0.f);
    const float sd = bfr(sqrtf(bfr(bfr(var) + 1e-5f)));
    const float rs = 1.f / sd;
    u32 o[2];
#pragma unroll
    for (int tl = 0; tl < 2; ++tl) {
      float hv[2];
#pragma unroll
      for (int e = 0; e < 2; ++e) {
        const float gt = bfr((acc[tl][tt][e] - m * S[tl][e]) * rs);
        const float up = bfr((acc[tl][tt][2 + e] - m * S[tl][2 + e]) * rs);
        const float sl = bfr(gt / (1.f + __expf(-gt)));
        hv[e] = sl * up;
      }
      o[tl] = pk_bf(hv[0], hv[1]);
    }
    const int tT = (t0 >> 4) + tt, ks3 = fg * 4 + (w >> 1);
    *(u32x2*)(hb + ((size_t)(tT * KS3 + ks3) * 64 + lane) * 8 + (w & 1) * 4) = u32x2{o[0], o[1]};
  }
  STAMP(2, 4);
}

constexpr int DN_ROW = 272, DN_WAVE = 64 * DN_ROW, DN_ST = 4 * DN_WAVE, DN_LDS = DN_ST + 4 * 64 * 8 + 64 * 144;
template <bool FINAL>
__global__ __launch_bounds__(256, 1) void k_down(const u16* __restrict__ xT, u16* __restrict__ outp, char* __restrict__ ws) {
  __shared__ __attribute__((aligned(16))) char sm[DN_LDS];
  const int tid = threadIdx.x, lane = tid & 63, w = __builtin_amdgcn_readfirstlane(tid >> 6);
  const int b = blockIdx.x;
  const int xcd = b & 7, idx = b >> 3;
  const int rt = xcd * 4 + (idx >> 3), ct = idx & 7;
  const u32x4* hp = (const u32x4*)(ws + WS_H) + (size_t)(rt * 4) * KS3 * 64 + lane;
  const u32x4* wp = (const u32x4*)(ws + WS_WD) + (size_t)(ct * 4) * KS3 * 64 + lane;

  STAMP(FINAL ? 6 : 3, 0);
  u32x4 hf[11][4], wf[11][4];
#pragma unroll
  for (int i = 0; i < 11; ++i) {
    const int ks = min(4 * i + w, KS3 - 1);
#pragma unroll
    for (int j = 0; j < 4; ++j) {
      hf[i][j] = hp[(size_t)(j * KS3 + ks) * 64];
      wf[i][j] = wp[(size_t)(j * KS3 + ks) * 64];
    }
  }
  const int tq = lane & 7, dsub = lane >> 3;
  const int tg = rt * 64 + tq * 8;
  u32x4 res[2];
#pragma unroll
  for (int i = 0; i < 2; ++i) res[i] = *(const u32x4*)(xT + (size_t)(ct * 64 + w * 16 + i * 8 + dsub) * XP + tg);
  u32x4 pv;
  if (!FINAL) pv = *(const u32x4*)(xT + (size_t)(ct * 64) * XP + tg);
  __builtin_amdgcn_sched_barrier(0);

  f32x4 acc[4][4];
#pragma unroll
  for (int i = 0; i < 4; ++i)
#pragma unroll
    for (int j = 0; j < 4; ++j) acc[i][j] = f32x4{0.f, 0.f, 0.f, 0.f};
#pragma unroll
  for (int i = 0; i < 11; ++i) {
    if (i < 10 || w < 3) {
#pragma unroll
      for (int tt = 0; tt < 4; ++tt)
#pragma unroll
        for (int dt = 0; dt < 4; ++dt)
          acc[tt][dt] = __builtin_amdgcn_mfma_f32_16x16x32_bf16(__builtin_bit_cast(b16x8, hf[i][tt]), __builtin_bit_cast(b16x8, wf[i][dt]), acc[tt][dt], 0, 0, 0);
    }
  }
  STAMP(FINAL ? 6 : 3, 1);
  {
    const int r16 = lane & 15, g = lane >> 4;
#pragma unroll
    for (int tt = 0; tt < 4; ++tt)
#pragma unroll
      for (int dt = 0; dt < 4; ++dt)
        *(f32x4*)(sm + w * DN_WAVE + (dt * 16 + r16) * DN_ROW + (tt * 16 + g * 4) * 4) = acc[tt][dt];
  }
  __syncthreads();
  STAMP(FINAL ? 6 : 3, 2);
  float s1[8], s2[8];
#pragma unroll
  for (int j = 0; j < 8; ++j) { s1[j] = 0.f; s2[j] = 0.f; }
#pragma unroll
  for (int i = 0; i < 2; ++i) {
    const int dl = w * 16 + i * 8 + dsub;
    f32x4 a0 = {0.f, 0.f, 0.f, 0.f}, a1 = {0.f, 0.f, 0.f, 0.f};
#pragma unroll
    for (int ww = 0; ww < 4; ++ww) {
      a0 += *(const f32x4*)(sm + ww * DN_WAVE + dl * DN_ROW + tq * 32);
      a1 += *(const f32x4*)(sm + ww * DN_WAVE + dl * DN_ROW + tq * 32 + 16);
    }
    float o[8];
#pragma unroll
    for (int j = 0; j < 8; ++j) {
      const float xr = (j & 1) ? bf_hi(res[i][j >> 1]) : bf_lo(res[i][j >> 1]);
      o[j] = bfr(xr + bfr(j < 4 ? a0[j & 3] : a1[j & 3]));
      if (!FINAL) {
        const float e = o[j] - ((j & 1) ? bf_hi(pv[j >> 1]) : bf_lo(pv[j >> 1]));
        s1[j] += e; s2[j] += e * e;
      }
    }
    if (!FINAL) {
      *(u32x4*)(outp + (size_t)(ct * 64 + dl) * XP + tg) =
          u32x4{pk_bf(o[0], o[1]), pk_bf(o[2], o[3]), pk_bf(o[4], o[5]), pk_bf(o[6], o[7])};
    } else {
      u16* tb = (u16*)(sm + DN_ST + 4 * 64 * 8);
#pragma unroll
      for (int j = 0; j < 8; ++j) tb[(tq * 8 + j) * 72 + dl] = (u16)(__builtin_bit_cast(u32, o[j]) >> 16);
    }
  }
  if (!FINAL) {
#pragma unroll
    for (int j = 0; j < 8; ++j) {
#pragma unroll
      for (int o = 8; o < 64; o <<= 1) { s1[j] += __shfl_xor(s1[j], o); s2[j] += __shfl_xor(s2[j], o); }
    }
    if (dsub == 0) {
#pragma unroll
      for (int j = 0; j < 8; ++j) *(f32x2*)(sm + DN_ST + (w * 64 + tq * 8 + j) * 8) = f32x2{s1[j], s2[j]};
    }
    __syncthreads();
    if (tid < 64) {
      float a1 = 0.f, a2 = 0.f;
#pragma unroll
      for (int ww = 0; ww < 4; ++ww) {
        const f32x2 v = *(const f32x2*)(sm + DN_ST + (ww * 64 + tid) * 8);
        a1 += v[0]; a2 += v[1];
      }
      const float pivot = bf2f(xT[(size_t)(ct * 64) * XP + rt * 64 + tid]);
      const float mean = pivot + a1 * (1.f / 64.f);
      const float m2 = fmaxf(a2 - a1 * a1 * (1.f / 64.f), 0.f);
      ((f32x2*)(ws + WS_SP))[(size_t)ct * L_ + rt * 64 + tid] = f32x2{mean, m2};
    }
  } else {
    __syncthreads();
    const u16* tb = (const u16*)(sm + DN_ST + 4 * 64 * 8);
#pragma unroll
    for (int i = 0; i < 2; ++i) {
      const int c = tid + 256 * i, tr = c >> 3, dc = c & 7;
      const u32x4 v = *(const u32x4*)(tb + tr * 72 + dc * 8);
      *(u32x4*)(outp + (size_t)(rt * 64 + tr) * D_ + ct * 64 + dc * 8) = v;
    }
  }
  STAMP(FINAL ? 6 : 3, 3);
}

extern "C" void kernel_launch(void* const* d_in, const int* in_sizes, int n_in, void* d_out, int out_size,
                              void* d_ws, size_t ws_size, hipStream_t stream) {
  const u16* x = (const u16*)d_in[0];
  const u16* lar = (const u16*)d_in[1];
  const u16* aim = (const u16*)d_in[2];
  const u16* cre = (const u16*)d_in[3];
  const u16* cim = (const u16*)d_in[4];
  const u16* ldt = (const u16*)d_in[5];
  const u16* dsk = (const u16*)d_in[6];
  const u16* w1 = (const u16*)d_in[8];
  const u16* w3 = (const u16*)d_in[9];
  const u16* w2 = (const u16*)d_in[10];
  char* ws = (char*)d_ws;
  u16* xt0 = (u16*)(ws + WS_XT0);
  u16* xt1 = (u16*)(ws + WS_XT1);
  u16* xt2 = (u16*)(ws + WS_XT2);
  u16* xt3 = (u16*)(ws + WS_XT3);
  k0_prep<<<256, 512, 0, stream>>>(x, lar, aim, cre, cim, ldt, w1, w3, w2, ws);
  k_conv<false><<<256, 256, 0, stream>>>(xt0, xt1, ws, dsk);
  k_gu<<<242, 512, 0, stream>>>(xt1, ws);
  k_down<false><<<256, 256, 0, stream>>>(xt1, xt2, ws);
  k_conv<true><<<256, 256, 0, stream>>>(xt2, xt3, ws, dsk);
  k_gu<<<242, 512, 0, stream>>>(xt3, ws);
  k_down<true><<<256, 256, 0, stream>>>(xt3, (u16*)d_out, ws);
}
```

```cpp
#include <hip/hip_runtime.h>
#include <hip/hip_bf16.h>
#include <stdint.h>

typedef unsigned short u16;
typedef unsigned int u32;
typedef short s16x4 __attribute__((ext_vector_type(4)));
typedef u32 u32x2 __attribute__((ext_vector_type(2)));
typedef u32 u32x4 __attribute__((ext_vector_type(4)));
typedef _Float16 h16x2 __attribute__((ext_vector_type(2)));
typedef _Float16 h16x8 __attribute__((ext_vector_type(8)));
typedef __bf16 b16x2 __attribute__((ext_vector_type(2)));
typedef __bf16 b16x8 __attribute__((ext_vector_type(8)));
typedef float f32x2 __attribute__((ext_vector_type(2)));
typedef float f32x4 __attribute__((ext_vector_type(4)));

#define DEV __device__ __forceinline__
#define ST_DECL
#define STAMP(i)
#define ST_SAVE(set, first)
#define ST_FLUSH()

#define CSTAMP(i) STAMP(i)
#define TSTAMP(i)
#define GSTAMP(i)
#define HSTAMP(i)
#define NSTAMP(i) STAMP(i)
#define DSTAMP(i)
#define FSTAMP(i) STAMP(i)
constexpr int L_ = 2048, D_ = 512, N_ = 128, F_ = 1376;
constexpr int GT = 11;

constexpr int MB = 1 << 20;
constexpr int WS_F2 = 11 * MB - 4096;
constexpr int WS_S = 11 * MB;
constexpr int WS_WGU = 12 * MB, WS_WD = 16 * MB;
constexpr int WS_H = 18 * MB;
constexpr int WS_SR = WS_H + 4 * MB;
constexpr int WS_FL = WS_SR + 16384;
constexpr int WS_HR = WS_FL + 4096;
constexpr int WS_X1 = 24 * MB, WS_X2 = 26 * MB, WS_X3 = 28 * MB;
constexpr int WS_P = 30 * MB;
#define S2_R 8
constexpr int WS_S2 = WS_P + 524288;

DEV float bf_lo(u32 v) { return __builtin_bit_cast(float, v << 16); }
DEV float bf_hi(u32 v) { return __builtin_bit_cast(float, v & 0xffff0000u); }
DEV float bf2f(u16 v) { return __builtin_bit_cast(float, (u32)v << 16); }
DEV u32 pk_bf(float a, float b) {
  f32x2 z = {a, b};
  return __builtin_bit_cast(u32, __builtin_convertvector(z, b16x2));
}
DEV float bfr(float a) { return bf_lo(pk_bf(a, a)); }
DEV u32 pk_h(float a, float b) {
  h16x2 z = {(_Float16)a, (_Float16)b};
  return __builtin_bit_cast(u32, z);
}
DEV u32 perm_lo(u32 a, u32 b) { return __builtin_amdgcn_perm(b, a, 0x05040100u); }
DEV u32 perm_hi(u32 a, u32 b) { return __builtin_amdgcn_perm(b, a, 0x07060302u); }

#define SB() __builtin_amdgcn_sched_barrier(0)
typedef __amdgpu_buffer_rsrc_t rsrc_t;
DEV rsrc_t mk_rsrc(const void* p) { return __builtin_amdgcn_make_buffer_rsrc(const_cast<void*>(p), (short)0, 0x10000000, 0x00020000); }
DEV u32x4 bld(rsrc_t r, int voff, int soff) { return __builtin_amdgcn_raw_buffer_load_b128(r, voff, soff, 0); }
DEV u32x2 bld2(rsrc_t r, int voff, int soff) { return __builtin_amdgcn_raw_buffer_load_b64(r, voff, soff, 0); }
DEV f32x4 mfma_bf(u32x4 a, u32x4 b, f32x4 c) {
  return __builtin_amdgcn_mfma_f32_16x16x32_bf16(__builtin_bit_cast(b16x8, a), __builtin_bit_cast(b16x8, b), c, 0, 0, 0);
}
DEV f32x4 mfma_h(u32x4 a, u32x4 b, f32x4 c) {
  return __builtin_amdgcn_mfma_f32_16x16x32_f16(__builtin_bit_cast(h16x8, a), __builtin_bit_cast(h16x8, b), c, 0, 0, 0);
}
template <int AUX> DEV void bst(u32x4 v, rsrc_t r, int voff) { __builtin_amdgcn_raw_buffer_store_b128(v, r, voff, 0, AUX); }
#define LDS_BARRIER() asm volatile("s_waitcnt lgkmcnt(0)\n\ts_barrier" ::: "memory")
DEV u32 pk_h_rtz(float a, float b) { return __builtin_bit_cast(u32, __builtin_amdgcn_cvt_pkrtz(a, b)); }

template <int CTRL> DEV float dpp_add(float v) {
  return v + __builtin_bit_cast(float, __builtin_amdgcn_update_dpp(0, __builtin_bit_cast(int, v), CTRL, 0xf, 0xf, true));
}
DEV float wave_total(float v) {
  v = dpp_add<0xb1>(v);
  v = dpp_add<0x4e>(v);
  v = dpp_add<0x141>(v);
  v = dpp_add<0x140>(v);
  const int iv = __builtin_bit_cast(int, v);
  const float a = __builtin_bit_cast(float, __builtin_amdgcn_readlane(iv, 0)), b = __builtin_bit_cast(float, __builtin_amdgcn_readlane(iv, 16));
  const float c = __builtin_bit_cast(float, __builtin_amdgcn_readlane(iv, 32)), d = __builtin_bit_cast(float, __builtin_amdgcn_readlane(iv, 48));
  return (a + b) + (c + d);
}
DEV f32x2 cmulv(f32x2 a, f32x2 b) {
  const f32x2 t = f32x2{a[0], a[0]} * b;
  return __builtin_elementwise_fma(f32x2{a[1], a[1]}, f32x2{-b[1], b[0]}, t);
}
struct Mode { float zr2, rh, rl; };
DEV f32x2 cexp_p(const Mode& m, float p) {
  const float mag = __builtin_amdgcn_exp2f(m.zr2 * p);
  const float ph = __builtin_fmaf(m.rl, p, __builtin_amdgcn_fractf(m.rh * p));
  return f32x2{mag * __builtin_amdgcn_cosf(ph), mag * __builtin_amdgcn_sinf(ph)};
}
struct ExpTab { u16 v[2056]; };
constexpr ExpTab make_exptab() {
  ExpTab t{};
  for (int i = 0; i < 2048; ++i) {
    const int l = 2047 - i;
    const int k = l < 256 ? 0 : l < 512 ? 1 : l < 1024 ? 2 : 3;
    const int p = k ? ((l + (1 << (k - 1)) - 1 + ((l >> k) & 1)) & ~((1 << k) - 1)) : l;
    const int xr = 8 * k + ((((p >> 7) << 3) | (p & 7)) >> k);
    t.v[i] = (u16)((((p >> 3) & 15) * 52 + xr) * 2);
  }
  for (int i = 2048; i < 2056; ++i) t.v[i] = 48 * 2;
  return t;
}
__constant__ __attribute__((aligned(16))) const ExpTab exptab = make_exptab();
constexpr int KC_PITCH = 4160;
constexpr int CV_KC = 0, CV_U = 2 * 4 * KC_PITCH, CV_UP = (256 + 2048) * 2;
constexpr int FF_X = 43008;
constexpr int CV_Y = FF_X + 65536;
constexpr int KG_ROW = 528;
constexpr int KG_CH = 64 * KG_ROW;
constexpr int KG_T = 81920;
constexpr int KG_P = KG_T + 2 * 1664;
constexpr int KG_C = KG_P + 4 * 176 * 4;
constexpr int KG_W = KG_C + 2 * 2560, KG_WP = 3200;
constexpr int FF_RS = FF_X;
constexpr int FF_T = FF_X + 24 * 4096;
constexpr int FF_DUMP = FF_T + 1536;
constexpr int LD_SV = FF_DUMP + 1536;
constexpr int LD_Q = LD_SV + 768;
constexpr int LD_KS = LD_Q + 16384;
constexpr int LD_STAMP = LD_KS + 16;
constexpr int LD_ALL = LD_STAMP + 1024;
static_assert(2 * KG_CH <= KG_T && CV_U + 2 * CV_UP <= FF_X && KG_W + 4 * KG_WP <= FF_T && LD_ALL <= 163840 && !((FF_T | LD_SV) & 255), "");
DEV u32x4 bldc(rsrc_t r, int voff, int soff) { return __builtin_amdgcn_raw_buffer_load_b128(r, voff, soff, 16); }
template <int I> struct IC { static constexpr int v = I; };
template <int B, int E, class F> DEV void sfor(F&& f) {
  if constexpr (B < E) { f(IC<B>{}); sfor<B + 1, E>(f); }
}

template <u32 V> DEV u32 fresh() { u32 v; asm volatile("v_mov_b32 %0, %1" : "=v"(v) : "i"(V)); return v; }
DEV void kernel_copies(char* sm, int kch, int ki, const u32 (&wd)[6]) {
  u32 al[5];
#pragma unroll
  for (int j = 0; j < 5; ++j) al[j] = __builtin_amdgcn_alignbit(wd[j + 1], wd[j], 16);
  char* base = sm + CV_KC + kch * 4 * KC_PITCH + ki * 16;
  *(u32x4*)(base + 0 * KC_PITCH) = u32x4{wd[0], wd[1], wd[2], wd[3]};
  *(u32x4*)(base + 1 * KC_PITCH) = u32x4{al[0], al[1], al[2], al[3]};
  *(u32x4*)(base + 2 * KC_PITCH) = u32x4{wd[1], wd[2], wd[3], wd[4]};
  *(u32x4*)(base + 3 * KC_PITCH) = u32x4{al[1], al[2], al[3], al[4]};
}
DEV void zero_margins(char* sm, int tid) {
  const int a = (tid & 64) ? CV_KC + ((tid >> 2) & 7) * KC_PITCH + 4096 + (tid & 3) * 16 : CV_U + ((tid >> 5) & 1) * CV_UP + (tid & 31) * 16;
  *(u32x4*)(sm + a) = u32x4{fresh<0u>(), fresh<0u>(), fresh<0u>(), fresh<0u>()};
}
template <bool FIRST> DEV void mma_h(f32x4& c, const u32x4& a, const u32x4& b) {
  if constexpr (FIRST) asm volatile("v_mfma_f32_16x16x32_f16 %0, %1, %2, 0" : "=&v"(c) : "v"(a), "v"(b));
  else asm volatile("v_mfma_f32_16x16x32_f16 %0, %1, %2, %0" : "+v"(c) : "v"(a), "v"(b));
}
struct TzOp { signed char r, s, j; };
struct TzPlan { TzOp op[144]; };
constexpr TzPlan tz_plan() {
  TzPlan p{};
  int n = 0;
  for (int ph = 0; ph < 4; ++ph)
    for (int r = 0; r < 4; ++r)
      for (int a = 0; a < 4; ++a)
        for (int c = 3; c >= 0; --c) {
          if (ph == 1 ? c >= a : c < a) continue;
          if (ph == 0) p.op[n++] = TzOp{(signed char)r, (signed char)(c - a), (signed char)a};
          if (ph == 1) p.op[n++] = TzOp{(signed char)r, (signed char)(4 + c - a), (signed char)a};
          if (ph == 2) p.op[n++] = TzOp{(signed char)r, (signed char)(4 + a), (signed char)(c - a)};
          if (ph == 3) p.op[n++] = TzOp{(signed char)r, (signed char)(c - a), (signed char)(4 + a)};
        }
  return p;
}
constexpr TzPlan TZ = tz_plan();
constexpr bool tz_first(int m) { for (int i = 0; i < m; ++i) if (TZ.op[i].s + TZ.op[i].j == TZ.op[m].s + TZ.op[m].j) return false; return true; }
constexpr int tz_last(int c) { int l = 0; for (int i = 0; i < 144; ++i) if (TZ.op[i].s + TZ.op[i].j == c) l = i; return l; }
#define TZ_B2 45
#define TZ_B3 96
#define TZ_BP 5
#define TZ_A2 40
DEV u32x2 tz_a(const char* abase, int r, int s, int h) { return *(const u32x2*)(abase + 64 * (3 - r + 24 - 8 * (s & 3)) + 8 * h); }
template <class F> DEV void toeplitz(char* sm, const u32x4& q0, u32x2 (&An)[4][4][2], F&& side) {
  const char* abase;
  const char* bbase = sm + q0[1];
  char* ybase = sm + q0[2];
  f32x4 acc[8];
  u32x2 Af[4][4][2];
  u32x4 B[4][8];
  auto rdb = [&](int n, int hi) { B[n >> 2][4 * hi + (n & 3)] = *(const u32x4*)(bbase + 512 * (4 * hi + (n & 3)) + 64 * (3 - (n >> 2))); SB(); };
#pragma unroll
  for (int n = 0; n < TZ_BP; ++n) rdb(n, 0);
  sfor<0, 144>([&](auto M_) {
    constexpr int m = decltype(M_)::v, r = TZ.op[m].r, s = TZ.op[m].s, j = TZ.op[m].j;
    const u32x2 (&h)[2] = s < 4 ? An[r][s & 3] : Af[r][s & 3];
    mma_h<tz_first(m)>(acc[s + j], u32x4{h[0][0], h[0][1], h[1][0], h[1][1]}, B[r][j]);
    SB();
    if constexpr (!(m & 1) && TZ_BP + m / 2 < 16) rdb(TZ_BP + m / 2, 0);
    if constexpr (m == TZ_A2 - 3) { int o = q0[0] - 2048; asm volatile("" : "+v"(o)); abase = sm + o; SB(); }
    sfor<0, 16>([&](auto N_) {
      constexpr int n = decltype(N_)::v, at = n < 8 ? TZ_A2 + 2 * n : 56 + 2 * n + 2 * (n >> 2);
      if constexpr (at == m) {
        Af[n >> 2][n & 3][0] = tz_a(abase, n >> 2, n & 3, 0);
        SB();
      }
      if constexpr (at + 1 == m) {
        Af[n >> 2][n & 3][1] = tz_a(abase, n >> 2, n & 3, 1);
        SB();
      }
    });
    sfor<0, 7>([&](auto K_) {
      constexpr int k = decltype(K_)::v;
      if constexpr (m == tz_last(k) + 4) { *(f32x4*)(ybase + k * 1024) = acc[k]; SB(); }
    });
    side(M_);
    if constexpr (m >= TZ_B3 && m < TZ_B3 + 32) {
      constexpr int d = m - TZ_B3;
      if constexpr (d < 6) rdb(d, 1);
      else if constexpr (!(d & 1) && 3 + d / 2 < 16) rdb(3 + d / 2, 1);
    }
  });
  asm volatile("s_nop 7\n\ts_nop 1" ::: "memory");
  SB();
#pragma unroll
  for (int k = 5; k < 8; ++k) *(f32x4*)(ybase + k * 1024) = acc[k];
}
static_assert(tz_last(3) + 4 < TZ_B2 && tz_last(4) + 4 < 144 && tz_last(5) + 4 >= 144, "");
extern "C" __device__ unsigned long long dispatch_number() __asm("llvm.amdgcn.dispatch.id");

#define PXS 0, 2, 4, 6
#define GP0 3
#define GP1 4
#define FF_GA 4, 4, 3, 3, 2, 0, 4, 4, 4, 4, 0
#define FF_GB 4, 4, 4, 4, 0, 4, 4, 4, 4, 0, 0
#define FF_LW 4
#define FF_NT 30
#define FF_NR 30
#define FF_W2N 14
#define FF_HA 29, 32, 16, 8, 4
#define FF_HB 29, 32, 16, 8, 4
#define FF_BQ 8
#define FF_CW 12
#define FF_UC 8
#define FF_OCA 16
#define FF_OCB 8
#define FF_X3 8
#define FF_FX 11, 13, 14, 19, 12, 79
#define FF_SV 3
#define FF_OWA 90, 1, 11
#define FF_OWB 90, 1, 11
constexpr int FF_OW[2][3] = {{FF_OWA}, {FF_OWB}};
#define XC_W 5
#define HC_W 1
#define FF_PQ 175
#define FF_TQ 50
enum { A_XF, A_XQ, A_XW, A_XL, A_CK, A_WL, A_SV, A_W2, A_H, A_FI, A_VW, A_ST, A_TR, A_RS, A_PR, A_SR, A_U, A_HC, A_OW, A_OR, A_O, A_RL, A_RM, A_OP, A_TC, A_SS, A_F2, A_AL, A_AC };
constexpr int NSITE = 528 + 176, S_MID = 512, S_DN = 528;
constexpr int FF_NU = 12 * 17 + 3 * 7, N_ST = 22, N_PR = 35, N_RM = 12, N_OP = 11;
constexpr int ff_slot(int c) { return c < 24 ? c : c - 24; }
struct Plan {
  short first[NSITE + 1], code[1536];
  signed char stat[33], endw[32], cum[12], n0, xi[12], hi[23];
  short over, pw;
  bool ok;
};
constexpr int n_out(int F) { return F ? 55 : 70; }
constexpr int out_cost(int F, int n) { return !F && n >= 50 && n < 54 ? 12 : !F && n == 54 ? 8 : 4; }
constexpr bool out_store(int F, int n) { return F ? n == 50 : n == 54 || n == 55 || n == 65; }
constexpr int pr_need(int F, int n) {
  return n < 5 ? 8 : n < 11 ? 10 : n < 22 ? (F && n < 11 + FF_W2N / 4 ? -1 : n < 15 ? 11 : 16) : 24;
}
constexpr Plan make_plan(int F) {
  Plan p{};
  p.ok = true;
  const int gs[2][11] = {{FF_GA}, {FF_GB}}, hs[2][5] = {{FF_HA}, {FF_HB}};
  short fx[NSITE][8] = {};
  signed char nf[NSITE] = {}, cap[NSITE] = {};
  int gaps[32] = {}, T[16] = {}, rdy[32] = {};
  auto put = [&](int site, int type, int arg, int cost) { fx[site][nf[site]++] = (short)(type * 1024 + arg); cap[site] = (signed char)(cap[site] - cost); };
  for (int g = 0; g < 11; ++g) {
    p.cum[g + 1] = (signed char)(p.cum[g] + gs[F][g]);
    for (int c = p.cum[g]; c < p.cum[g + 1]; ++c) rdy[c] = g ? 2 * g + 1 : -1;
    if (p.cum[g + 1] < 32 && p.cum[g + 1] < 2 * g + 4) p.ok = false;
    if (p.cum[g] < 16 && p.cum[g + 1] > 16) p.ok = false;
  }
  if (p.cum[11] != 32) p.ok = false;
  for (int s = 0; s < 33; ++s) p.stat[s] = -1;
  for (int m = 0; m < 16; ++m) {
    T[m] = m ? (T[m - 1] > rdy[2 * m + 1] ? T[m - 1] : rdy[2 * m + 1]) + 1 : 0;
    p.stat[T[m]] = (signed char)m;
  }
  const int C0 = T[7] + 1, C1 = T[15] + 1, B0 = (C0 + 2) & ~1, B1 = C1 + 1, R0 = rdy[15];
  if (B0 > 14 || B1 > 30) p.ok = false;
  for (int s = 0; s < 32; ++s) {
    gaps[s] = p.stat[s] >= 0 ? 16 : 12;
    for (int g = 0; g < gaps[s]; ++g) cap[16 * s + g] = s < 16 ? 8 : FF_UC;
    p.endw[s] = (s && !(s & 1) && s <= 20 && gs[F][s / 2]) || s == B0 || s == B1 ? -1 : 0;
    for (int i = 0; i < 4 && s < 31; ++i) put(16 * s + i, A_XF, 4 * (s + 1) + i, 4);
    if (p.stat[s + 1] >= 0) { put(16 * s + 5, A_XQ, 2 * p.stat[s + 1], 4); put(16 * s + 7, A_XQ, 2 * p.stat[s + 1] + 1, 4); }
  }
  for (int q = 0; q < 176; ++q) cap[S_DN + q] = 8;
  int RB = R0;
  while (p.endw[RB] >= 0) ++RB;
  int cs[11] = {};
  for (int g = 1; g < 11; ++g) {
    const int n = gs[F][g];
    cs[g] = 2 * g - 2;
    if (!n) continue;
    const int c0 = g == 1 ? 5 : gaps[cs[g]] - n - 2;
    put(16 * cs[g] + c0 - 1, A_VW, 512 + g, 4);
    for (int j = 0; j < n + 2; ++j) put(16 * cs[g] + c0 + j, A_CK, 8 * g + j, j > n ? 8 : 4);
    int s = g == 1 ? 0 : 2 * g - 1, gap = g == 1 ? 12 : 4;
    for (int j = 0; j < 2 * n; ++j) {
      put(16 * s + gap, A_XW, 2 * p.cum[g] + j, FF_CW);
      if (s > 2 * g) p.ok = false;
      if (p.cum[g] + j / 2 >= 24 && (s <= RB || s <= T[3] || RB < 7)) p.ok = false;
      gap += 2;
      if (gap >= gaps[s]) { ++s; gap = 4; }
    }
  }
  put(16 * R0 + 9, A_RS, 0, 4); put(16 * R0 + 11, A_RS, 1, 4);
  for (int j = 0; j < 3; ++j) { put(16 * 15 + 7 + 2 * j, A_TR, j, 4); put(16 * 31 + 2 * j, A_TR, 4 + j, 4); }
  if (F) for (int j = 0; j < 3; ++j) put(16 * 13 + 7 + 2 * j, A_SS, 4 + j, 4);
  const int fxs[6] = {FF_FX};
  if (!F && fxs[0] >= 0) {
    for (int j = 0; j < 3; ++j) put(16 * fxs[j] + 3, A_FI, 2 + j, 8);
    put(16 * fxs[3] + 3, A_VW, 255, 0); put(16 * fxs[3] + 3, A_F2, 0, 8);
    put(S_DN + fxs[4], A_AL, 0, 4);
    for (int j = 0; j < 5; ++j) put(S_DN + fxs[5] + (j + 2) / 3, A_AC, j, j == 4 ? 8 : j == 3 ? 64 : 4);
    if ((fxs[5] & 7) != 7 || fxs[5] > 90 || fxs[0] <= FF_X3) p.ok = false;
  }
  int nx = 0, nl = F ? 48 : FF_NT, ns = F ? 3 : 0, n2 = F ? FF_W2N : 0, nh = 0, ni = F ? 2 : 0, svs = 0;
  auto grp = [&](int c) { int g = 0; while (p.cum[g + 1] <= c) ++g; return g; };
  while (grp(nx) == 0 || cs[grp(nx)] - FF_LW < 0) ++nx;
  p.n0 = (signed char)nx;
  for (int s = 0; s < 32; ++s)
    for (int gap = 1; gap < gaps[s]; gap += 4) {
      const int site = 16 * s + gap;
      if (nx < 32 && s >= cs[grp(nx)] - FF_LW) put(site, A_XL, nx++, 4);
      else if (ni < 2 && s >= FF_X3 + 2 * ni) put(site, A_FI, ni++, 4);
      else if (ns < 3 && s >= (FF_SV < 0 ? R0 : FF_SV)) { put(site, A_SV, ns++, 4); svs = s; }
      else if (nl < 48) put(site, A_WL, nl++, 4);
      else if (nh < 16 && s >= hs[F][nh >> 3] && n2 >= 36) put(site, A_H, nh++, 4);
      else if (n2 < 44 && s >= 11 && (F || s >= rdy[17 + 2 * ((16 * (n2 >> 2) + 15) / 22)]) && n2 < FF_W2N + 3 * (s - 16)) put(site, A_W2, n2++, 4);
      else if (n2 < FF_W2N && s >= 11 && s >= rdy[17 + 2 * ((16 * (n2 >> 2) + 15) / 22)]) put(site, A_W2, n2++, 4);
    }
  if (!F) {
    for (int k = FF_NT / 3; k < 16; ++k) put(16 * (k - 1) + gaps[k - 1] - 1, A_VW, 256 + k, 4);
    put(16 * (svs + 3), A_VW, 256, 4);
  }
  if (nx < 32 || nl < 48 || ns < 3 || n2 + 6 < 44) p.ok = false;
  while (n2 < 44) put(S_MID, A_W2, n2++, 4);
  while (nh < 8 && nf[S_MID] < 8) put(S_MID, A_H, nh++, 4);
  const int hl = nh;
  for (int e = 0; e < 6; ++e) while (nh < hl + ((e + 1) * (hs[F][2] - hl)) / 6) put(S_MID + 2 + e, A_H, nh++, 4);
  put(S_MID + 8, A_VW, 768, 4);
  for (int j = 0; j < 4; ++j) put(S_MID + 8, A_HC, j, 4);
  const int OQ = 8 * FF_OW[F][2] + 8;
  for (int q = 0; q < 176; ++q) {
    const int k = q >> 3, n = q & 7;
    if (k < 21 && n == 2) put(S_DN + q, A_VW, 768 + k + 1, 4);
    if (k < 21 && n >= 3 && n < 7) put(S_DN + q, A_HC, 4 * (k + 1) + n - 3, n == 6 ? 8 : 4);
    if (nh < 44 && q >= hs[F][3] && (q - hs[F][3]) % hs[F][4] == 0) { if (q + 16 > 4 * nh) p.ok = false; put(S_DN + q, A_H, nh++, 4); }
    if (q == 1 || q == 9) put(S_DN + q, A_RS, 2 + q / 8, 4);
    if (q >= FF_OW[F][0] && q < FF_OW[F][0] + 6 * FF_OW[F][1] && (q - FF_OW[F][0]) % FF_OW[F][1] == 0) {
      put(S_DN + q, A_OW, (q - FF_OW[F][0]) / FF_OW[F][1], 2 * FF_CW);
      if (q >= OQ || q < 90) p.ok = false;
    }
    if (q >= OQ && q < OQ + 3) { put(S_DN + q, A_OR, 2 * (q - OQ), 4); put(S_DN + q, A_OR, 2 * (q - OQ) + 1, 4); }
    if (!F && q == FF_PQ) put(S_DN + q, A_RL, 0, 4);
    if (!F && q == FF_TQ) put(S_DN + q, A_TC, 0, 4);
  }
  short uc[FF_NU] = {};
  int nu = 0;
  for (int j = 0; j <= 12; ++j) {
    const int nd = j == 0 ? 0 : (j & 3) ? 1 : 8;
    for (int st = 0; st < 16; ++st) {
      if (j < 12) uc[nu++] = (short)(32 * j + st);
      if (st < nd) uc[nu++] = (short)(32 * (j - 1) + 16 + st);
    }
  }
  int u = 0, st0 = 0, st1 = 0, pr = 0, sr = F ? 51 : 0, op = 0, o = 0;
  for (int site = 0; site < NSITE; ++site) {
    const int s = site >> 4, gap = site & 15;
    if (site < S_MID) {
      if (gap >= gaps[s]) { if (nf[site]) p.ok = false; continue; }
      if (s >= C0 && st0 < N_ST) { put(site, A_ST, st0, st0 == 14 || st0 == 18 ? 8 : 4); ++st0; if (s > B0) p.ok = false; }
      if (s >= C1 && st1 < N_ST) { put(site, A_ST, 32 + st1, st1 == 14 || st1 == 18 ? 8 : 4); ++st1; if (s > B1) p.ok = false; }
      while (pr < N_PR && pr_need(F, pr) < 0) ++pr;
      while (pr < N_PR && nf[site] < 8 && (cap[site] >= 4 || pr_need(F, pr) <= s + (gap >= 6)) && (s < 16 || pr_need(F, pr) <= s + 2) && (pr < 34 || s > R0)) {
        put(site, A_PR, pr++, 4);
        while (pr < N_PR && pr_need(F, pr) < 0) ++pr;
      }
      while (sr < 51 && site > 16 * (svs + 3) && nf[site] < 8 && (cap[site] >= 4 || (s == 15 && cap[site] > -12))) {
        if (sr < 48) put(site, A_SR, sr, 4); else put(site, A_SS, sr - 48, 2 * FF_CW);
        ++sr;
      }
      if (s >= 16 && sr < 48) p.ok = false;
      while (site >= 258 && u < FF_NU && cap[site] >= 4 && nf[site] < 8) {
        const int st = uc[u] & 31;
        if (st == 23 && s < rdy[F ? 15 : 31]) break;
        put(site, A_U, uc[u++], st == 4 || st == 5 || st == 10 || st == 11 ? 8 : 4);
      }
    } else if (site >= S_DN) {
      const int q = site - S_DN;
      while (q >= 12 && op < N_OP && cap[site] >= 4) put(site, A_OP, op++, 4);
      if (q >= OQ + 5) cap[site] = (signed char)(cap[site] + (F ? FF_OCB : FF_OCA) - 8);
      while (q >= OQ + 5 && o < n_out(F) && cap[site] >= 4 && nf[site] < 8) { put(site, A_O, o, out_cost(F, o)); ++o; }
    }
  }
  if (op < N_OP || o < n_out(F) || pr < N_PR) p.ok = false;
  p.over = (short)(FF_NU - u);
  int n = 0, vm = 0, px[32] = {}, pl[48] = {}, p2[44] = {}, ph[44] = {}, psv = 0, ppv = 0, pfi = 0, pal = 0, vc = 0, done = 2 - p.n0, xi = p.n0, hi = 0, ld = 0;
  for (int c = 0; c < p.n0; ++c) px[c] = c - p.n0 - 1;
  for (int c = 0; c < 48; ++c) pl[c] = -100;
  for (int c = 0; c < 44; ++c) p2[c] = -100;
  for (int site = 0; site < NSITE; ++site) {
    p.first[site] = (short)n;
    if (site == S_MID + 1) for (; u < FF_NU; ++u) { p.code[n++] = (short)(A_U * 1024 + uc[u]); vm += (uc[u] & 31) == 23; }
    for (int j = 0; j < nf[site]; ++j) {
      const int t = fx[site][j] >> 10, a = fx[site][j] & 1023;
      if (t == A_VW) {
        int need = a >= 768 ? ph[2 * (a - 768) + 1] : a >= 512 ? px[p.cum[a - 511] - 1] : a > 256 ? pl[3 * (a - 256) + 2] : a == 255 ? pfi : psv;
        if (a >= 768 && a < 779 && p2[4 * (a - 768) + 3] > need) need = p2[4 * (a - 768) + 3];
        if (HC_W && a > 768) {
          const int c = vm - vc + p.hi[a - 769] - 2 * (a - 768) - 2;
          if (vm - 1 - c > need) need = vm - 1 - c;
          done = need > done ? need : done;
          p.code[n++] = (short)(A_VW * 1024 + vm - 1 - need);
          continue;
        }
        if (need <= done) continue;
        done = need;
        if (vm - 1 - need > 63) continue;
        p.code[n++] = (short)(A_VW * 1024 + vm - 1 - need);
        continue;
      }
      p.code[n++] = fx[site][j];
      if (t == A_XL) { px[a] = vm; xi = a + 1; }
      if (t == A_WL) pl[a] = vm;
      if (t == A_W2) p2[a] = vm;
      if (t == A_H) { ph[a] = vm; hi = a + 1; }
      if (t == A_SV) psv = vm;
      if (t == A_RL) ppv = vm;
      if (t == A_FI) pfi = vm;
      if (t == A_AL) pal = vm;
      if ((t == A_F2 && done < pfi) || (t == A_AC && done < pal)) p.ok = false;
      if (t == A_CK) p.xi[a >> 3] = (signed char)xi;
      if (t == A_CK && ((XC_W >> (1 + F)) & 1) && (a & 7) == p.cum[(a >> 3) + 1] - p.cum[a >> 3] + 1) {
        const int c1 = p.cum[(a >> 3) + 1];
        for (int c = c1; c < xi; ++c) px[c] = vm - (xi - c);
        if (done > vm - (xi - c1) - 1) done = vm - (xi - c1) - 1;
      }
      if (t == A_HC) { p.hi[a >> 2] = (signed char)hi; vc = vm; }
      vm += t == A_XL || t == A_WL || t == A_W2 || t == A_H || t == A_SV || t == A_FI || t == A_RL || t == A_TC || t == A_F2 || t == A_AL || (t == A_U && (a & 31) == 23) ||
            (t == A_O && out_store(F, a));
      if (t == A_XF || t == A_XQ || t == A_TR || (t == A_SS && a >= 4) || (t == A_PR && a == 0)) ld = 0;
      if (t == A_XW || t == A_RS || (t == A_SS && a < 4) || (t == A_ST && ((a & 31) == 11 || (a & 31) == 19 || (a & 31) == 21))) ++ld;
    }
    if (site < S_MID && (site & 15) == 15 && p.endw[site >> 4] == 0) p.endw[site >> 4] = (signed char)ld;
    if (site >= S_MID + 2 && site < S_MID + 8 && ((site - S_MID) & 1)) ++vm;
  }
  p.first[NSITE] = (short)n;
  p.pw = (short)(vm - 1 - ppv);
  return p;
}
constexpr Plan PLAN[2] = {make_plan(0), make_plan(1)};
static_assert(FF_PQ > 162 && PLAN[0].ok && PLAN[1].ok && FF_NR + FF_W2N == 44, "");

template <bool FIRST> DEV void mma_w(f32x4& c, const u32x4& a, const u32x4& b) {
  if constexpr (FIRST) asm volatile("v_mfma_f32_16x16x32_bf16 %0, %1, %2, 0" : "=&v"(c) : "a"(a), "v"(b));
  else asm volatile("v_mfma_f32_16x16x32_bf16 %0, %1, %2, %0" : "+v"(c) : "a"(a), "v"(b));
}
template <bool FIRST> DEV void mma_v(f32x4& c, const u32x4& a, const u32x4& b) {
  if constexpr (FIRST) asm volatile("v_mfma_f32_16x16x32_bf16 %0, %1, %2, 0" : "=&v"(c) : "v"(a), "v"(b));
  else asm volatile("v_mfma_f32_16x16x32_bf16 %0, %1, %2, %0" : "+v"(c) : "v"(a), "v"(b));
}
DEV void mma_s(f32x4& c, const u32x2& a, const u32x2& b) { asm volatile("v_mfma_f32_16x16x16_bf16 %0, %1, %2, 0" : "=&v"(c) : "v"(a), "v"(b)); }
#define MFMA_DRAIN() do { asm volatile("s_nop 7\n\ts_nop 1" ::: "memory"); SB(); } while (0)
#define LGKM0() do { __builtin_amdgcn_s_waitcnt(0xc07f); SB(); } while (0)
#define VM0() __builtin_amdgcn_s_waitcnt(0x0f70)
#define WG_BARRIER() do { asm volatile("" ::: "memory"); __builtin_amdgcn_s_waitcnt(0xc07f); __builtin_amdgcn_s_barrier(); asm volatile("" ::: "memory"); SB(); } while (0)
DEV void pin(float& v) { asm volatile("" : "+v"(v)); }
DEV void pinv(int& v) { asm volatile("" : "+v"(v)); }
DEV void pinu(u32& v) { asm volatile("" : "+v"(v)); }
DEV void pins(int& v) { asm volatile("" : "+s"(v)); }
DEV float as_f(u32 v) { return __builtin_bit_cast(float, v); }
DEV u32 as_u(float v) { return __builtin_bit_cast(u32, v); }
DEV u32 max3u(u32 a, u32 b, u32 c) { return max(max(a, b), c); }
DEV u32 no_fill(u32 v) { return min(v, 0xfffffffeu); }
DEV u32 ln_stat(float s1, float s2) {
  const float mean = s1 * (1.f / D_);
  const float var = fmaxf(s2 * (1.f / D_) - mean * mean, 0.f);
  return pk_bf(mean, __builtin_amdgcn_sqrtf(bfr(bfr(var) + 1e-5f)) * (1.f / 1024.f));
}
#define CB_SL 1
template <int ID> DEV void cold_begin() {
  asm volatile("s_cbranch_vccnz .Lca%0\n\t.subsection 1\n.Lcb%0:\n\ts_sleep %1\n\t.subsection 0" :: "i"(ID), "i"(CB_SL));
}
template <int OFF> DEV void cold_load(u32x4& d, int voff, rsrc_t rs, int soff) {
  asm volatile(".subsection 1\n\tbuffer_load_dwordx4 %0, %1, %2, %3 offen offset:%4 sc1\n\t.subsection 0" : "+v"(d) : "v"(voff), "s"(rs), "s"(soff), "i"(OFF));
}
#define COLD_END "s_cbranch_vccz .Lcc%[id]\n\ts_sub_i32 %[tr], %[tr], 1\n\ts_cmp_gt_i32 %[tr], 0\n\ts_cbranch_scc1 .Lcb%[id]\n\ts_branch .Lcc%[id]\n" \
                 ".Lca%[id]:\n\ts_movk_i32 %[tr], 0x2000\n\ts_branch .Lcb%[id]\n\t.subsection 0\n.Lcc%[id]:"
template <int ID, int Y> DEV void cold_end_x(int tr, u32 m, const u32x4& a, const u32x4& b, const u32x4& c, const u32x4& d) {
  asm volatile(".subsection 1\n\ts_waitcnt vmcnt(%[y])\n\tv_max3_u32 %0, %1, %2, %3\n\tv_max3_u32 %0, %0, %4, %5\n\tv_max3_u32 %0, %0, %6, %7\n\t"
               "v_max_u32 %0, %0, %8\n\tv_cmp_eq_u32 vcc, -1, %0\n\t" COLD_END
               :: "v"(m), "v"(a[0]), "v"(a[2]), "v"(b[0]), "v"(b[2]), "v"(c[0]), "v"(c[2]), "v"(d[0]), "v"(d[2]), [tr] "s"(tr), [id] "i"(ID), [y] "i"(Y) : "scc");
}
template <int ID, int F, int Y> DEV void cold_end_h(int tr, u32 m, const u32x4& a, const u32x4& b) {
  asm volatile(".subsection 1\n\ts_waitcnt vmcnt(%[y])\n\tv_bitop3_b32 %0, %1, %2, %3 bitop3:%5\n\tv_bitop3_b32 %0, %0, %4, 1 bitop3:%6\n\t"
               "v_cmp_ne_u32 vcc, %7, %0\n\t" COLD_END
               :: "v"(m), "v"(a[0]), "v"(a[2]), "v"(b[0]), "v"(b[2]), "i"(F ? 0x80 : 0xfe), "i"(F ? 0x80 : 0xa8), "i"(F), [tr] "s"(tr), [id] "i"(ID), [y] "i"(Y) : "scc");
}
#define CF_(n) "v_bitop3_b32 %0, %0, %" #n ", %17 bitop3:0xf6\n\t"
template <int ID> DEV void cold_end_f(int tr, u32 m, const u32x4 (&a)[4], u32 tag) {
  asm volatile(".subsection 1\n\ts_waitcnt vmcnt(0)\n\tv_bitop3_b32 %0, %1, %2, %17 bitop3:0x7e\n\t" CF_(3) CF_(4) CF_(5) CF_(6) CF_(7) CF_(8) CF_(9) CF_(10)
               CF_(11) CF_(12) CF_(13) CF_(14) CF_(15) CF_(16) "v_cmp_ne_u32 vcc, 0, %0\n\t" COLD_END
               :: "v"(m), "v"(a[0][0]), "v"(a[0][1]), "v"(a[0][2]), "v"(a[0][3]), "v"(a[1][0]), "v"(a[1][1]), "v"(a[1][2]), "v"(a[1][3]),
               "v"(a[2][0]), "v"(a[2][1]), "v"(a[2][2]), "v"(a[2][3]), "v"(a[3][0]), "v"(a[3][1]), "v"(a[3][2]), "v"(a[3][3]), "s"(tag),
               [tr] "s"(tr), [id] "i"(ID) : "scc");
}
template <int ID> DEV void cold_end_g(int tr, u32 m, const u32x4& a, u32 tag) {
  asm volatile(".subsection 1\n\ts_waitcnt vmcnt(0)\n\tv_bitop3_b32 %0, %1, %2, %5 bitop3:0x7e\n\tv_bitop3_b32 %0, %0, %3, %5 bitop3:0xf6\n\t"
               "v_bitop3_b32 %0, %0, %4, %5 bitop3:0xf6\n\tv_cmp_ne_u32 vcc, 0, %0\n\t" COLD_END
               :: "v"(m), "v"(a[0]), "v"(a[1]), "v"(a[2]), "v"(a[3]), "s"(tag), [tr] "s"(tr), [id] "i"(ID) : "scc");
}
DEV void useu(u32 v) { asm volatile("" :: "v"(v)); }
DEV void w_ready(u32& fl, u32 tag, int voff, rsrc_t rs, int soff) {
  int tr;
  asm volatile("s_waitcnt vmcnt(0)\n\tv_cmp_ne_u32 vcc, %[tag], %[fl]\n\ts_cbranch_vccnz .Lwa%=\n.Lwc%=:\n\t.subsection 1\n.Lwa%=:\n\ts_movk_i32 %[tr], 0x2000\n.Lwb%=:\n\ts_sleep 1\n\t"
               "buffer_load_dword %[fl], %[vo], %[rs], %[so] offen sc1\n\ts_waitcnt vmcnt(0)\n\tv_cmp_ne_u32 vcc, %[tag], %[fl]\n\t"
               "s_cbranch_vccz .Lwc%=\n\ts_sub_i32 %[tr], %[tr], 1\n\ts_cmp_gt_i32 %[tr], 0\n\ts_cbranch_scc1 .Lwb%=\n\ts_branch .Lwc%=\n\t.subsection 0"
               : [fl] "+v"(fl), [tr] "=&s"(tr) : [tag] "s"(tag), [vo] "v"(voff), [rs] "s"(rs), [so] "s"(soff) : "vcc", "scc", "memory");
  SB();
}
template <int OFF> DEV void x_load(u32x4& d, int voff, rsrc_t rs, int soff) {
  asm volatile("buffer_load_dwordx4 %0, %1, %2, %3 offen offset:%4 sc1" : "=v"(d) : "v"(voff), "s"(rs), "s"(soff), "i"(OFF));
}
template <int OFF> DEV void a_load(u32x4& d, int voff, rsrc_t rs, int soff) {
  asm volatile("buffer_load_dwordx4 %0, %1, %2, %3 offen offset:%4 sc1" : "=a"(d) : "v"(voff), "s"(rs), "s"(soff), "i"(OFF));
}
template <int OFF> DEV void v_load(f32x4& d, int voff, rsrc_t rs, int soff) {
  asm volatile("buffer_load_dwordx4 %0, %1, %2, %3 offen offset:%4 sc1" : "=v"(d) : "v"(voff), "s"(rs), "s"(soff), "i"(OFF));
}
#define RL_SL "1"
#define P_LD(j, b, o) "buffer_load_dwordx4 %" #j ", %8, %9, %" #b " offen offset:" #o " sc1\n\t"
DEV void p_load(unsigned long long lanes, u32x4 (&d)[8], int voff, rsrc_t rs, int s0, int s1) {
  asm volatile("s_mov_b64 exec, %12\n\ts_cbranch_execz .Lpl%=\n\t" P_LD(0, 10, 0) P_LD(1, 10, 1024) P_LD(2, 10, 2048) P_LD(3, 10, 3072)
               P_LD(4, 11, 0) P_LD(5, 11, 1024) P_LD(6, 11, 2048) P_LD(7, 11, 3072) "\n.Lpl%=:\n\ts_mov_b64 exec, -1"
               : "+v"(d[0]), "+v"(d[1]), "+v"(d[2]), "+v"(d[3]), "+v"(d[4]), "+v"(d[5]), "+v"(d[6]), "+v"(d[7])
               : "v"(voff), "s"(rs), "s"(s0), "s"(s1), "s"(lanes));
}
template <int Y> DEV void p_wait(u32x4 (&d)[8]) {
  asm volatile("s_waitcnt vmcnt(%8)" : "+v"(d[0]), "+v"(d[1]), "+v"(d[2]), "+v"(d[3]), "+v"(d[4]), "+v"(d[5]), "+v"(d[6]), "+v"(d[7]) : "i"(Y));
}
DEV void re_load(unsigned long long lanes, u32x4& d, u32x4& e, int voff, rsrc_t rs, int sd, int se) {
  asm volatile("s_sleep " RL_SL "\n\ts_mov_b64 exec, %6\n\tbuffer_load_dwordx4 %0, %2, %3, %4 offen sc1\n\tbuffer_load_dwordx4 %1, %2, %3, %5 offen sc1\n\t"
               "s_mov_b64 exec, -1\n\ts_waitcnt vmcnt(0)"
               : "+v"(d), "+v"(e) : "v"(voff), "s"(rs), "s"(sd), "s"(se), "s"(lanes) : "memory");
}
template <int Y> DEV void vm_wait(u32 v) { asm volatile("s_waitcnt vmcnt(%1)" :: "v"(v), "i"(Y) : "memory"); }
template <int Y> DEV void vm_wait() { asm volatile("s_waitcnt vmcnt(%0)" :: "i"(Y)); }
template <int Y> DEV void x_wait(u32x4& a) { asm volatile("s_waitcnt vmcnt(%1)" : "+v"(a) : "i"(Y)); }
template <int Y> DEV void x_wait(u32x4& a, u32x4& b) { asm volatile("s_waitcnt vmcnt(%2)" : "+v"(a), "+v"(b) : "i"(Y)); }
DEV unsigned long long absent(const u32x4& s, const u32x4& x) {
  return __builtin_amdgcn_ballot_w64(max3u(max3u(s[0], s[1], s[2]), s[3], max(x[0], x[2])) == ~0u);
}
DEV void pinl(unsigned long long& v) { asm volatile("" : "+s"(v)); }
constexpr int TRIES = 1 << 13;
#define CODE_LEN 58112
#define TOUCH_AT 0
#define TOUCH_N 1
#define W2_AT 2
#define W_AT 1
#define TZ_V0 12
#define TZ_VS 3
#define XF 2
#define E_AT 2
#define TLBW 0
#define WP_AT 1
#define TZ_FL 12
#define ZT3 0x7fff0000
#define FI_AT 2
#define TZ_A0 60
#define TZ_A1 99
#define FL_AT 2
#define PL_AT 0
#define POLL_AT 3
#define CODE_2 29056
#define TC_H 0
#define OUT_AUX 0
#define TZ_F0 -1
#define MAP 0
#define TL_SA 2
#define TL_SB 0
#define TL_R0 1
#define TL_R1 0
#define TZ_W0A 3
#define TZ_W1A 98
#define TZ_W0B 3
#define TZ_W1B 98
#define TZ_P1 8
#define TZ_P0 0
#define TZ_L0 50
#define TZ_L1 80
#define TZ_N0 4
#define TZ_X0A 108
#define TZ_X0B 108
#define TZ_XS 3
#define TZ_C0A 130
#define TZ_C0B 130
#define TZ_E0 66
#define TZ_E1 72
#define TZ_ED 0
#define TZ_ES 6
#define TZ_EN 14
constexpr int tz_wat(int F, int n) { return F ? TZ_W0B + n * (TZ_W1B - TZ_W0B) / (FF_NR - 1) : TZ_W0A + n * (TZ_W1A - TZ_W0A) / (FF_NT - 1); }
constexpr int tz_wl(int F, int m) { int n = 0; while (n < (F ? FF_NR : FF_NT) && tz_wat(F, n) <= m) ++n; return n; }
constexpr int tz_xat(int F, int n) { return (F ? TZ_X0B : TZ_X0A) + n * TZ_XS; }
constexpr int tz_xl(int F, int m) { int n = 0; while (n < PLAN[F].n0 && tz_xat(F, n) <= m) ++n; return n; }

__global__ __launch_bounds__(256, 1) void k_all(const u16* __restrict__ x, const u16* __restrict__ lar,
                                                const u16* __restrict__ aim, const u16* __restrict__ cre,
                                                const u16* __restrict__ cim, const u16* __restrict__ ldt,
                                                const u16* __restrict__ dskip, const u16* __restrict__ w1,
                                                const u16* __restrict__ w3, const u16* __restrict__ wdn,
                                                u16* __restrict__ outp, char* __restrict__ ws) {
  __shared__ __attribute__((aligned(16))) char sm[LD_ALL];
  const int b = blockIdx.x, tid0 = threadIdx.x;
  int w = __builtin_amdgcn_readfirstlane(tid0 >> 6);
  ST_DECL;
  STAMP(0);
  const rsrc_t rs = mk_rsrc(ws);
  const int pr = (b & 7) * 32 + (b >> 3);
  const u32x4 ones16 = {~0u, ~0u, ~0u, ~0u};
  u32x4 xv, sv, gl[4];
  float xu[2][4][2], ds[2];
  u32 z[2][4], wv[8];
  u32x4 wf[3][16], w2[11][4];
  u32x2 An[4][4][2];
  u32x4 q0, f16, pq[8];
  auto near = [&]() {
    const char* abase = sm + q0[0];
#pragma unroll
    for (int n = 0; n < 32; ++n) { An[n >> 3][3 - ((n >> 1) & 3)][n & 1] = tz_a(abase, n >> 3, 3 - ((n >> 1) & 3), n & 1); asm volatile("" ::: "memory"); }
    SB();
  };
  auto norm0 = [&](int tid, const u32 (&xk)[4], const u32 (&sk)[4]) {
    u32 c[4];
#pragma unroll
    for (int k = 0; k < 4; ++k) {
      const float mv = bf_lo(sk[k]), rv = __builtin_amdgcn_rcpf(bf_hi(sk[k]));
      xu[0][k][0] = bf_lo(xk[k]); xu[0][k][1] = bf_hi(xk[k]);
      const u32 d = pk_bf(xu[0][k][0] - mv, xu[0][k][1] - mv);
      c[k] = pk_bf(bf_lo(d) * rv, bf_hi(d) * rv);
    }
    char* up = sm + CV_U + 512 + tid * 8;
    *(u32x2*)up = u32x2{pk_h_rtz(bf_lo(c[0]), bf_lo(c[1])), pk_h_rtz(bf_lo(c[2]), bf_lo(c[3]))};
    *(u32x2*)(up + CV_UP) = u32x2{pk_h_rtz(bf_hi(c[0]), bf_hi(c[1])), pk_h_rtz(bf_hi(c[2]), bf_hi(c[3]))};
    SB();
#pragma unroll
    for (int k = 0; k < 4; ++k) { z[0][k] = pk_bf(bf_lo(c[k]) * ds[0], bf_hi(c[k]) * ds[1]); pinu(z[0][k]); }
    SB();
  };
  u32x4 o1;
  int wa;
  auto put_w2 = [&](int b, int tid) {
    const int cl = 22 * (b & 7) + (b >> 3);
    const u32 mk = (cl & 195) == 67 ? 0xffffu : ~0u;
    u32x4 o0;
#pragma unroll
    for (int k = 0; k < 4; ++k) {
      const u32 lo = perm_lo(wv[2 * k], wv[2 * k + 1]) & ((k & 1) ? mk : ~0u), hi = perm_hi(wv[2 * k], wv[2 * k + 1]) & ((k & 1) ? mk : ~0u);
      const u32 lx = __builtin_amdgcn_update_dpp(0, lo, 0x4e, 0xf, 0xf, true), hx = __builtin_amdgcn_update_dpp(0, hi, 0x4e, 0xf, 0xf, true);
      o0[k] = (tid & 2) ? hx : lo; o1[k] = (tid & 2) ? hi : lx;
    }
    const int wb = (b < 176 ? 0 : 0x70000000) + WS_WD + (((cl >> 2) & 3) * 11 + ((cl >> 4) & 15)) * 4096 + (cl & 3) * 256;
    wa = wb + (tid >> 5) * (44 * 4096) + ((tid >> 3) & 3) * 1024 + (tid & 4) * 32 + (tid & 1) * 32 + (tid & 2) * 8;
    bst<16>(o0, rs, wa);
    asm volatile("" : "+v"(o1));
    SB();
  };
  constexpr int fxs[6] = {FF_FX}, NFILL = fxs[0] >= 0 ? 2 : 5;
  auto fill = [&](int b, int tid, int j) {
    if (j == 0) f16 = u32x4{fresh<~0u>(), fresh<~0u>(), fresh<~0u>(), fresh<~0u>()};
    if (NFILL == 2) {
      bst<16>(f16, rs, (j == 0 || tid < 124 ? WS_HR + (b * 380 + j * 256) * 16 : 0x7fff0000) + tid * 16);
    } else {
      if (j < 2) bst<16>(f16, rs, WS_X2 + b * 8192 + w * 1024 + tid * 16 + j * 1024);
      if (j == 2) bst<16>(f16, rs, WS_HR + (b * 380 + tid) * 16);
      if (j == 3) bst<16>(f16, rs, (tid < 124 ? WS_HR + (b * 380 + 256) * 16 : WS_P + (b * 128 - 124) * 16) + tid * 16);
      if (j == 4) bst<16>(f16, rs, tid < 2 * S2_R ? WS_S2 + b * (32 * S2_R) + tid * 16 : 0x7ffffff0);
    }
    SB();
  };
  int S_wg, S_sv, S_fl, S_x, S_w2, S_h, S_rd, S_p, S_s2, S_x2, S_ln, S_tr, S_sr, S_f1, S_f2, S_f3;
  unsigned long long M_1, M_2, M_r[2], pc;
  auto places = [&]() {
    const int xcd = b & 7, idx = b >> 3;
    const int rt = MAP ? xcd * 2 + (idx >> 4) : (xcd >> 1) * 4 + (idx >> 3), hb = MAP ? (idx >> 3) & 1 : xcd & 1, ct = idx & 7, G = hb * 8 + ct;
    S_wg = WS_WGU + (G * GT + 3 * w) * 16384; S_sv = WS_S + (G * GT + 3 * w) * 1024; S_fl = WS_FL + G * 256;
    S_x = rt * 512 + w * 32768; S_w2 = WS_WD + (ct * 4 + w) * 11 * 4096; S_h = WS_H + ((rt * 2 + hb) * 8 + w) * 22 * 1024;
    S_rd = ct < 4 ? 65536 : -32768; S_p = WS_P + (((ct >> 2) * 16 + rt) * 2 + hb) * 8192; S_s2 = WS_S2 + (ct >> 2) * 4096 + (rt * 2 + hb) * 128; S_x2 = WS_X2 + pr * 8192;
    S_ln = CODE_2 + (idx * 4 + w) * 128;
    M_1 = 0xaaaaaaaaaaaaaaaaull; M_2 = 0xccccccccccccccccull;
    pins(S_wg); pins(S_sv); pins(S_fl); pins(S_x); pins(S_w2); pins(S_h); pins(S_rd); pins(S_p); pins(S_s2); pins(S_x2); pins(S_ln);
    S_tr = 0; pins(S_tr);
    S_f1 = WS_X2 + b * 8192 + w * 2048; S_f2 = WS_P + b * (2048 + 32 * S2_R) + w * 1024; S_f3 = WS_F2 + b * 16 + w * 4;
    pins(S_f1); pins(S_f2); pins(S_f3);
    S_sr = WS_S2 + (b & (S2_R - 1)) * 8192; pins(S_sr);
    asm volatile("s_cmp_eq_u32 %2, 0\n\ts_cselect_b64 %0, -1, 0\n\ts_cmp_eq_u32 %2, 16\n\ts_cselect_b64 %1, -1, 0" : "=&s"(M_r[0]), "=&s"(M_r[1]) : "s"(ct * 4 + w) : "scc");
    pinl(M_1); pinl(M_2);
    const int tid = tid0, l = tid & 63, r16 = l & 15, g = l >> 4, phi = (3 - r16) & 3, pm = ((r16 >> 1) + 8 * (r16 & 1)) * 16;
    char* qp = sm + LD_Q + tid * 16;
    q0 = u32x4{(u32)(CV_KC + (w >> 1) * 4 * KC_PITCH + phi * KC_PITCH + 2 * (2055 - r16 - 8 * g - phi) - 64 * ((w & 1) * 4 + 3 + 24)),
               (u32)(CV_U + (w >> 1) * CV_UP + 2 * (256 + 16 * r16 + 8 - 8 * g) - 64 * ((w & 1) * 4 + 3)),
               (u32)(CV_Y + w * 8192 + r16 * 64 + ((g ^ ((r16 >> 1) & 3)) * 16)), (u32)(CV_Y + w * 1024 + (l >> 2) * 64 + (((l ^ (l >> 3)) & 3) * 16))};
    *(u32x4*)qp = q0;
    *(u32x4*)(qp + 4096) = u32x4{(u32)(((l >> 3) & 3) * 8192 + ((l & 7) + 8 * (l >> 5)) * 16), (u32)(FF_X + w * 256 + (l >> 5) * 2048 + (l & 7) * 16 + ((l >> 3) & 3) * 4),
                                 (u32)(FF_X + g * 256 + pm), (u32)(FF_X + (2 * ct + (w >> 1)) * 4096 + 2 * hb * 1024 + (2 * (w & 1) + (g >> 1)) * 256 + pm + (g & 1) * 8)};
    u32x4 hv;
#pragma unroll
    for (int n = 0; n < 3; ++n) {
      const int cell = n < 2 ? n * 64 + G * 4 + w : 128 + 3 * G + w, kk = cell >> 2;
      hv[n] = (n == 2 && w == 3 ? 0x70000000 : WS_H + ((rt * 16 + (kk & 3)) * 22 + (kk >> 2) * 2) * 1024 + (cell & 3) * 256) + ((g >> 1) * 176 + (g & 1)) * 1024 + r16 * 16;
    }
    hv[3] = (r16 >> 2) == g ? FF_T + r16 * 16 + w * 4 : FF_DUMP + l * 4;
    *(u32x4*)(qp + 8192) = hv;
    *(u32x4*)(qp + 12288) = u32x4{(u32)(((r16 >> 2) == g ? 0 : 0x70000000) + WS_P + (rt * 2 + hb) * 8192 + (ct * 4 + w) * 256 + r16 * 16),
                                  (u32)(WS_X2 + (32 * ct + 8 * w) * 8192 + (2 * rt + hb) * 128 + g * 16384 + r16 * 8),
                                  (u32)((2 * rt + hb) * 32768 + ct * 128 + w * 32 + (2 * r16 + (g & 1)) * 1024 + (g >> 1) * 16),
                                  (u32)(WS_X1 + pr * 8192 + (tid >> 4) * 512 + r16 * 16)};
    SB();
  };
  u32 dsk, wfl;
  int fla;
  rsrc_t r2;
  int S_v2;
  const u32 tag = 0x7fd30001u | (((u32)dispatch_number() & 0x7fffu) << 1);
  {
    const int tid = tid0, lane = tid & 63;
    asm volatile("s_getpc_b64 %0" : "=s"(pc));
    pc &= ~255ull;
    u32 touch = 0u;
    auto req_code = [&]() {
      touch = *(const __attribute__((address_space(1))) u32*)(pc + min((TOUCH_N * 128 * (lane & 3) + (b >> 3) * 4 + w) * (128 / TOUCH_N) + (lane >> 2) % TOUCH_N * (16384 / TOUCH_N), CODE_LEN));
      SB();
    };
    if (TOUCH_AT == 0) req_code();
    SB();
    asm volatile("" :: "s"(x), "s"(lar), "s"(aim), "s"(cre), "s"(cim), "s"(ldt), "s"(dskip), "s"(w1), "s"(w3), "s"(wdn), "s"(outp), "s"(ws));
    const rsrc_t rx = mk_rsrc(x);
    const int e = w & 1, pi = (2 * pr + (w >> 1)) * (N_ * 2);
    u32 p_ldt, p_cre, p_cim, p_lar, p_aim;
    u32x4 xa, xb, xs0, xs1;
    auto req_p = [&]() {
      p_ldt = __builtin_amdgcn_raw_buffer_load_b32(mk_rsrc(ldt), 0, pr * 4, 0);
      p_cre = __builtin_amdgcn_raw_buffer_load_b32(mk_rsrc(cre), lane * 4, pi, 0);
      p_cim = __builtin_amdgcn_raw_buffer_load_b32(mk_rsrc(cim), lane * 4, pi, 0);
      p_lar = __builtin_amdgcn_raw_buffer_load_b32(mk_rsrc(lar), lane * 4, pi, 0);
      p_aim = __builtin_amdgcn_raw_buffer_load_b32(mk_rsrc(aim), lane * 4, pi, 0);
      SB();
    };
    auto req_x = [&]() {
      const int xo = ((tid & 3) * 4 * D_ + (tid >> 2) * 8) * 2, xbase = (b >> 1) * 16384 + (b & 1) * 2048;
      xa = bld(rx, xo, xbase); xb = bld(rx, xo + D_ * 2, xbase);
      const int so = ((lane & 1) * D_ + ((lane >> 1) & 7) * 64 + (lane >> 4) * 8) * 2;
      xs0 = bld(rx, so, xbase + w * 4096); xs1 = bld(rx, so + 64, xbase + w * 4096);
      SB();
    };
    u32 warm = 0u;
    auto req_t = [&]() {
      warm = __builtin_amdgcn_raw_buffer_load_b32(rs, WS_S + min(lane, 39) * 524288 + 520192 + w * 128, 0, 16);
      SB();
    };
    if (TLBW == 1) req_t();
    if (XF) req_x();
    if (TLBW == 2) req_t();
    req_p();
    if (TLBW == 3) req_t();
    if (TOUCH_AT == 1) req_code();
    if (!XF) req_x();
    const int tG = b & 15, ksb = b >> 4;
    u32x4 wg[4];
    auto req_w = [&]() {
      const rsrc_t r1 = mk_rsrc(w1), r3 = mk_rsrc(w3);
      const int wo = ((lane >> 4) * F_ + min((lane & 15) * 8, 78)) * 2, wso = ((ksb * 32 + w * 8) * F_ + tG * 86) * 2;
      wg[0] = bld(r1, wo, wso); wg[1] = bld(r3, wo, wso);
      wg[2] = bld(r1, wo, wso + 8 * F_); wg[3] = bld(r3, wo, wso + 8 * F_);
      SB();
    };
    if (W_AT == 0) req_w();
    const int cl = 22 * (b & 7) + (b >> 3), c3 = cl - 128, c33 = (c3 * 43) >> 7, cG = cl < 128 ? (cl >> 2) & 15 : c33;
    const int cc = cl < 128 ? 3 * (cl & 3) + (cl >> 6) : 3 * (c3 - 3 * c33) + 2;
    r2 = __builtin_amdgcn_make_buffer_rsrc(const_cast<u16*>(wdn), (short)0, b < 176 ? F_ * D_ * 2 : 0, 0x00020000);
    S_v2 = (86 * cG + 8 * cc) * (D_ * 2);
    auto req_w2 = [&]() {
      const int vo = S_v2 + tid * 4;
#pragma unroll
      for (int j = 0; j < 8; ++j) {
        const int jj = j >> 1, fl = 4 * (jj & 1) + 2 * (j & 1) + (jj >> 1);
        wv[j] = __builtin_amdgcn_raw_buffer_load_b32(r2, vo + fl * 1024, 0, 0);
      }
      SB();
    };
    SB();
    if (W2_AT == 0) req_w2();
    u32x4 et;
    u32x2 et2;
    const u16* const etp = exptab.v;
    auto req_e = [&]() {
      et = *(const u32x4*)(etp + tid * 8);
      et2 = *(const u32x2*)(etp + tid * 8 + 8);
      dsk = __builtin_amdgcn_raw_buffer_load_b32(mk_rsrc(dskip), 0, pr * 4, 0);
      SB();
    };
    if (E_AT == 0) req_e();
    auto set_x1 = [&]() {
      bst<16>(ones16, rs, WS_X1 + pr * 8192 + w * 2048 + lane * 16);
      bst<16>(ones16, rs, WS_X1 + pr * 8192 + w * 2048 + lane * 16 + 1024);
      SB();
    };
    if (FI_AT == 0) set_x1();
    if (W_AT == 2) req_w();
    if (PL_AT == 0) places();
    if (W_AT == 5) req_w();
    STAMP(1);
    Mode md[2];
    f32x2 c0[2], E[5][2];
    auto consts = [&]() {
      const float dt = bfr(expf((w >> 1) ? bf_hi(p_ldt) : bf_lo(p_ldt)));
      const float bnd = (fabsf(bf_lo(p_cre) * dt) + fabsf(bf_lo(p_cim) * dt)) + (fabsf(bf_hi(p_cre) * dt) + fabsf(bf_hi(p_cim) * dt));
      int ex;
      (void)frexpf(2.f * wave_total(bnd), &ex);
      ex = min(max(ex, -100), 100);
      const float sc = ldexpf(1.f, 15 - ex);
#pragma unroll
      for (int k = 0; k < 2; ++k) {
        const float ar = -bfr(expf(k ? bf_hi(p_lar) : bf_lo(p_lar)));
        const float zi = -(k ? bf_hi(p_aim) : bf_lo(p_aim)) * dt;
        const float rh = zi * 0.159154943f;
        md[k] = Mode{(ar * dt) * 1.44269504089f, rh, __builtin_fmaf(zi, 6.4206383e-9f, __builtin_fmaf(zi, 0.159154943f, -rh))};
        c0[k] = f32x2{((k ? bf_hi(p_cre) : bf_lo(p_cre)) * dt) * sc, (-(k ? bf_hi(p_cim) : bf_lo(p_cim)) * dt) * sc};
      }
      *(float*)(sm + LD_KS + (w >> 1) * 4) = ldexpf(1.f, ex - 15 - 10);
      *(u32*)(sm + KG_T + (w >> 1) * 1664 + 96) = 0u;
      SB();
    };
    if (XF < 2) consts();
    if (W_AT == 6) req_w();
    STAMP(2);
    auto powers = [&](int j0, int j1) {
#pragma unroll
      for (int j = j0; j < j1; ++j) {
        const float p = j == 0 ? 128.f : j == 1 ? (e ? 2.f : 1.f) : j == 2 ? (e ? 256.f : 8.f) : j == 3 ? (e ? 1024.f : 64.f) : 512.f;
        E[j][0] = cexp_p(md[0], p); E[j][1] = cexp_p(md[1], p);
      }
      SB();
    };
    if (XF < 2) powers(0, GP0);
    auto put_x = [&](int q) {
      const int go = WS_H + b * 16384 + (tid & 3) * 16;
      bst<16>(u32x4{tag, xa[q], tag, xb[q]}, rs, go + ((2 * (tid >> 2) + (q >> 1) + b) & 127) * 128 + (q & 1) * 64);
      SB();
    };
    f32x4 s1, s2;
    auto sum_st = [&]() {
      const u32x4 ones = {0x3f803f80u, 0x3f803f80u, 0x3f803f80u, 0x3f803f80u};
      const f32x4 zr = {0.f, 0.f, 0.f, 0.f};
      s2 = mfma_bf(xs1, xs1, mfma_bf(xs0, xs0, zr)); s1 = mfma_bf(xs1, ones, mfma_bf(xs0, ones, zr));
      SB();
    };
    auto put_st = [&]() {
      const float d01 = (lane & 1) ? s2[1] : s2[0], d23 = (lane & 1) ? s2[3] : s2[2];
      float q2 = ((lane >> 2) & 3) == (lane >> 4) ? ((lane & 2) ? d23 : d01) : 0.f;
      float q1 = (lane & 1) ? s1[1] + s1[3] : s1[0] + s1[2];
      q2 = dpp_add<0x122>(q2); q2 = dpp_add<0x124>(q2); q2 = dpp_add<0x128>(q2);
      const auto a = __builtin_amdgcn_permlane16_swap(as_u(q1), as_u(q2), false, false);
      u32 t = as_u(as_f(a[0]) + as_f(a[1]));
      const auto c = __builtin_amdgcn_permlane32_swap(t, t, false, false);
      t = as_u(as_f(c[0]) + as_f(c[1]));
      const auto f = __builtin_amdgcn_permlane16_swap(t, t, false, false);
      __builtin_amdgcn_raw_buffer_store_b64(u32x2{tag, ln_stat(as_f(f[0]), as_f(f[1]))}, rs, lane < 2 ? WS_SR + (b & 1) * 8192 + ((b >> 1) * 8 + w * 2 + lane) * 8 : 0x7ffffff0, 0, 16);
      SB();
    };
    auto px = [&](int at) {
      constexpr int pos[4] = {PXS};
#pragma unroll
      for (int q = 0; q < 4; ++q) if (pos[q] == at) put_x(q);
    };
    px(0);
    sum_st();
    px(1);
    if (XF < 2) powers(GP0, GP1);
    put_st();
    px(2);
    if (W_AT == 1) req_w();
    if (E_AT == 1) req_e();
    if (FI_AT == 1) set_x1();
    if (W2_AT == 1) req_w2();
    if (TOUCH_AT == 2) req_code();
    if (PL_AT == 1) places();
    STAMP(3);
    u32x4 wo[4];
    int wdst[2];
    auto st_w = [&](int j) { bst<16>(wo[j], rs, wdst[j >> 1] + (j & 1) * 128); SB(); };
    auto put_w = [&]() {
      char* st = sm + KG_W + w * KG_WP;
#pragma unroll
      for (int j = 0; j < 4; ++j) *(u32x4*)(st + (j & 1) * 1600 + ((j >> 1) * 4 + (lane >> 4)) * 192 + min(lane & 15, 11) * 16) = wg[j];
#pragma unroll
      for (int h = 0; h < 2; ++h) {
        const int r = min(h * 64 + lane, 87), fo = (r & ~3) + 2 * (r & 1);
        const char* rp = st + ((r >> 1) & 1) * 1600 + min(fo < 80 ? fo : fo + 2, 86) * 2;
        const u32 mk = fo < 86 ? ~0u : 0u;
        u32 ta[8];
#pragma unroll
        for (int j = 0; j < 8; ++j) ta[j] = *(const u32*)(rp + j * 192) & mk;
        const u32x4 o0 = {perm_lo(ta[0], ta[1]), perm_lo(ta[2], ta[3]), perm_lo(ta[4], ta[5]), perm_lo(ta[6], ta[7])};
        const u32x4 o1 = {perm_hi(ta[0], ta[1]), perm_hi(ta[2], ta[3]), perm_hi(ta[4], ta[5]), perm_hi(ta[6], ta[7])};
        const int tdst = h * 64 + lane < 88 ? WS_WGU + ((tG * GT + (r >> 3)) * 16 + ksb) * 1024 + w * 256 + (r & 7) * 16 : 0x7ffff000;
        if (FL_AT < 7) {
          bst<16>(o0, rs, tdst);
          bst<16>(o1, rs, tdst + 128);
        } else {
          wo[2 * h] = o0; wo[2 * h + 1] = o1; wdst[h] = tdst;
          asm volatile("" : "+v"(wo[2 * h]), "+v"(wo[2 * h + 1]), "+v"(wdst[h]));
        }
        float s0 = 0.f, s1 = 0.f;
#pragma unroll
        for (int j = 0; j < 8; ++j) { s0 += bf_lo(ta[j]); s1 += bf_hi(ta[j]); }
        float* pp = (float*)(sm + KG_P) + w * 176 + (r >> 3) * 16 + (r & 7);
        pp[0] = s0; pp[8] = s1;
      }
      SB();
    };
    auto put_s = [&]() {
      if (w == 3) {
        const f32x4* pp = (const f32x4*)(sm + KG_P) + min(lane, 43);
        const f32x4 sum = (pp[0] + pp[44]) + (pp[88] + pp[132]);
        bst<16>(__builtin_bit_cast(u32x4, sum), rs, lane < 44 ? WS_S + tG * GT * 1024 + lane * 256 + ksb * 16 : 0x7ffffff0);
      }
      SB();
    };
    if (XF == 2) consts();
    px(3);
    if (W_AT == 3) req_w();
    if (XF == 2) powers(0, 2);
    px(4);
    powers(XF < 2 ? GP1 : 2, 5);
    px(5);
    if (W_AT == 4) req_w();
    if (WP_AT == 0) { put_w(); if (FI_AT == 5) set_x1(); if (W2_AT == 5) req_w2(); }
    STAMP(4);
    {
      char* tb = sm + (w >> 1) * KG_CH + lane * 8;
      auto put = [&](int row, f32x2 a, f32x2 c) { *(u32x2*)(tb + row * KG_ROW) = u32x2{pk_h(a[0], a[1]), pk_h(c[0], c[1])}; };
      if (e == 0) {
        f32x2 s[2], g0[2], g1[2], g2[2], g3[2];
#pragma unroll
        for (int k = 0; k < 2; ++k) {
          g0[k] = c0[k]; g1[k] = cmulv(c0[k], E[0][k]);
          s[k] = f32x2{E[2][k][0], -E[2][k][1]};
          g2[k] = f32x2{16384.f, 0.f}; g3[k] = f32x2{16384.f * E[3][k][0], -16384.f * E[3][k][1]};
        }
#pragma unroll
        for (int y = 0; y < 8; ++y) {
          if (y == 4) { SB(); px(6); }
          put(y, g0[0], g0[1]); put(8 + y, g1[0], g1[1]); put(48 + y, g2[0], g2[1]); put(56 + y, g3[0], g3[1]);
          if (y < 7) {
#pragma unroll
            for (int k = 0; k < 2; ++k) {
              g0[k] = cmulv(g0[k], E[1][k]); g1[k] = cmulv(g1[k], E[1][k]);
              g2[k] = y ? cmulv(g2[k], s[k]) : 16384.f * s[k]; g3[k] = cmulv(g3[k], s[k]);
            }
          }
        }
      } else {
        f32x2 g[2], f[2], h[2], e4[2], e6[2];
#pragma unroll
        for (int k = 0; k < 2; ++k) {
          g[k] = cmulv(c0[k], E[2][k]); f[k] = cmulv(c0[k], E[3][k]);
          e4[k] = cmulv(E[1][k], E[1][k]); e6[k] = cmulv(e4[k], E[1][k]);
          h[k] = cmulv(g[k], E[4][k]);
        }
#pragma unroll
        for (int c = 0; c < 9; ++c) {
          if (c == 2) { SB(); px(6); }
          put(32 + c, f[0], f[1]);
          if (c < 8) { f[0] = cmulv(f[0], E[0][0]); f[1] = cmulv(f[1], E[0][1]); }
          if (c < 2) {
            put(16 + 4 * c, g[0], g[1]);
            put(17 + 4 * c, cmulv(g[0], E[1][0]), cmulv(g[1], E[1][1]));
            put(18 + 4 * c, cmulv(g[0], e4[0]), cmulv(g[1], e4[1]));
            put(19 + 4 * c, cmulv(g[0], e6[0]), cmulv(g[1], e6[1]));
          } else if (c < 4) {
            put(20 + 2 * c, g[0], g[1]);
            put(21 + 2 * c, cmulv(g[0], e4[0]), cmulv(g[1], e4[1]));
          } else if (c < 6) {
            put(20 + 2 * c, h[0], h[1]);
            put(21 + 2 * c, cmulv(h[0], e4[0]), cmulv(h[1], e4[1]));
          }
          if (c < 3) { g[0] = cmulv(g[0], E[0][0]); g[1] = cmulv(g[1], E[0][1]); }
          if (c == 4) { h[0] = cmulv(h[0], E[0][0]); h[1] = cmulv(h[1], E[0][1]); }
        }
      }
    }
    SB();
    px(7);
    HSTAMP(5);
    if (E_AT == 2) req_e();
    if (FI_AT == 2) set_x1();
    if (W2_AT == 2) req_w2();
    if (WP_AT == 1) put_w();
    if (FI_AT == 3) set_x1();
    if (E_AT == 3) req_e();
    if (W2_AT == 3) req_w2();
    NSTAMP(5); HSTAMP(6);
    LDS_BARRIER();
    auto flag = [&](auto Y_) {
      vm_wait<decltype(Y_)::v>(touch + warm);
      __builtin_amdgcn_raw_buffer_store_b32(tag, rs, lane == 0 ? WS_FL + tG * 256 + (ksb * 4 + w) * 4 : 0x7ffffff0, 0, 16);
      SB();
    };
    if (FF_SV >= 0) put_s();
    if (FL_AT == 0) flag(IC<0>{});
    HSTAMP(7); GSTAMP(1);
    if (PL_AT == 2) places();
    u32x4 ga[4], sa[4];
    int gxo[2], gso = WS_SR + tid * 16;
#pragma unroll
    for (int a = 0; a < 2; ++a) { const int bb = 2 * (tid >> 2) + a; gxo[a] = WS_H + bb * 16384 + (((pr >> 1) + bb) & 127) * 128 + (pr & 1) * 64 + (tid & 3) * 16; }
    auto poll1 = [&](int n) {
      const int q = n >> 1;
      if (n & 1) sa[q] = bldc(rs, gso + (q >> 1) * 2048, (q & 1) * 8192 + (q >> 1) * 2048);
      else ga[q] = bldc(rs, gxo[q & 1], (q >> 1) * 2 * MB);
      SB();
    };
    auto poll = [&]() {
      asm volatile("" : "+v"(gxo[0]), "+v"(gxo[1]), "+v"(gso));
#pragma unroll
      for (int n = 0; n < 8; ++n) poll1(n);
    };
    {
      const int r16 = lane & 15, g = lane >> 4, ch = w >> 1;
      const char* ap = sm + ch * KG_CH + (e * 32 + r16) * KG_ROW + (g & 1) * 256 + (g >> 1) * 128;
      const char* bp = sm + ch * KG_CH + (48 + r16) * KG_ROW + (g & 1) * 256 + (g >> 1) * 128;
      f32x4 acc = {0.f, 0.f, 0.f, 0.f}, acc2 = {0.f, 0.f, 0.f, 0.f};
      u32x4 a0[8], a1[8], bh[8];
#pragma unroll
      for (int ks = 0; ks < 8; ++ks) {
        a0[ks] = *(const u32x4*)(ap + ks * 16); bh[ks] = *(const u32x4*)(bp + ks * 16);
        a1[ks] = *(const u32x4*)(ap + 16 * KG_ROW + ks * 16);
      }
      SB();
      GSTAMP(2);
      if (POLL_AT == 0) poll();
#pragma unroll
      for (int ks = 0; ks < 8; ++ks) {
        if (POLL_AT == 4 && !(ks & 1)) { SB(); poll1(ks >> 1); }
        acc = mfma_h(a0[ks], bh[ks], acc);
        acc2 = mfma_h(a1[ks], bh[ks], acc2);
      }
      acc *= 1.f / 8192.f; acc2 *= 1.f / 8192.f;
      char* tp = sm + KG_T + ch * 1664 + (r16 * 52 + e * 32 + g * 4) * 2;
      *(u32x2*)tp = u32x2{pk_h(acc[0], acc[1]), pk_h(acc[2], acc[3])};
      if (e == 0) *(u32x2*)(tp + 32) = u32x2{pk_h(acc2[0], acc2[1]), pk_h(acc2[2], acc2[3])};
    }
    SB();
    GSTAMP(3);
    if (PL_AT == 4) places();
    if (FL_AT == 2) flag(IC<(POLL_AT == 0 ? 8 : POLL_AT == 4 ? 4 : 0) + (W2_AT == 3 ? 8 : 0) + (E_AT == 3 ? 3 : 0)>{});
    if (W2_AT == 6) req_w2();
    if (FF_SV < 0) put_s();
    GSTAMP(4);
    if (POLL_AT == 1) poll();
    if (POLL_AT == 3) { poll1(0); poll1(1); poll1(2); poll1(3); }
    if (POLL_AT == 4) { poll1(4); poll1(5); poll1(6); poll1(7); }
    GSTAMP(5);
    LDS_BARRIER();
    NSTAMP(6); GSTAMP(6);
    {
      u32 wd[2][6];
#pragma unroll
      for (int ch = 0; ch < 2; ++ch) {
        const char* tp = sm + KG_T + ch * 1664;
        if (POLL_AT == 2 || POLL_AT == 3) { SB(); poll1(2 * ch + 4 * (POLL_AT - 2)); }
        if (FL_AT == 7) { SB(); st_w(2 * ch); }
#pragma unroll
        for (int j = 0; j < 6; ++j) {
          const u32 o = j < 4 ? et[j] : et2[j - 4];
          wd[ch][j] = (u32)*(const u16*)(tp + (o & 0xffffu)) | ((u32)*(const u16*)(tp + (o >> 16)) << 16);
          if ((POLL_AT == 2 || POLL_AT == 3) && j == 2) { SB(); poll1(2 * ch + 1 + 4 * (POLL_AT - 2)); }
          if (FL_AT == 7 && j == 2) { SB(); st_w(2 * ch + 1); }
        }
        if (FL_AT >= 5 && FL_AT == 5 + ch) { SB(); flag(IC<POLL_AT == 3 ? 2 * FL_AT - 4 : POLL_AT == 2 ? 2 * FL_AT - 8 : 8>{}); }
        if (POLL_AT != 2) kernel_copies(sm, ch, tid, wd[ch]);
      }
      if (POLL_AT == 2) {
        SB(); poll1(4);
        kernel_copies(sm, 0, tid, wd[0]);
        SB(); poll1(5);
        kernel_copies(sm, 1, tid, wd[1]);
        SB(); poll1(6);
      }
    }
    zero_margins(sm, tid);
    if (POLL_AT == 2) { SB(); poll1(7); }
    if (FI_AT == 4 || FI_AT == 6) set_x1();
    if (FL_AT == 7) { fla = lane == 0 ? WS_FL + tG * 256 + (ksb * 4 + w) * 4 : 0x7ffffff0; pinv(fla); useu(touch); }
    SB();
    GSTAMP(7);
    if (FL_AT == 1) flag(IC<8>{});
    if (PL_AT == 3) places();
    LDS_BARRIER();
    near();
    NSTAMP(7);
    auto late = [&]() {
      u32 t = 0u;
#pragma unroll
      for (int q = 0; q < 4; ++q)
        t |= __builtin_amdgcn_bitop3_b32(ga[q][0], ga[q][2], tag, 0x7e) | __builtin_amdgcn_bitop3_b32(sa[q][0], sa[q][2], tag, 0x7e);
      return __builtin_amdgcn_ballot_w64(t != 0u) != 0ull;
    };
    ds[0] = bf_lo(dsk) * (1.f / 1024.f); ds[1] = bf_hi(dsk) * (1.f / 1024.f);
    pin(ds[0]); pin(ds[1]);
    if (__builtin_expect(late(), 0)) {
      int tr = 0;
#pragma unroll 1
      do {
        asm volatile("s_sleep 2" ::: "memory"); poll(); VM0();
      } while (late() && ++tr < TRIES);
    }
    SB();
    CSTAMP(8);
    if (FL_AT == 3) flag(IC<0>{});
    if (TZ_F0 < 0) asm volatile("buffer_load_dword %0, %1, %2, %3 offen sc1" : "=v"(wfl) : "v"(lane * 4), "s"(rs), "s"(S_fl));
    SB();
    norm0(tid, {ga[0][1], ga[0][3], ga[1][1], ga[1][3]}, {sa[0][1], sa[0][3], sa[1][1], sa[1][3]});
    gl[0] = ga[2]; gl[1] = ga[3]; gl[2] = sa[2]; gl[3] = sa[3];
    ST_SAVE(0, 0);
  }
  auto layer = [&](auto FIN_) {
    constexpr int fin = decltype(FIN_)::v;
    constexpr const Plan& P = PLAN[fin];
    constexpr int xbuf = WS_X1 + fin * (WS_X3 - WS_X1), NW = fin ? FF_NR : FF_NT;
    int tid = tid0;
    pinv(tid);
    const int lane = tid & 63, lv = lane * 16;
    f32x4 Sv[3];
    u32x4 xr[32];
    int xl[4], sx[4];
    auto load_x = [&](auto N_) { constexpr int n = decltype(N_)::v; x_load<(n >> 4) * 256>(xr[n], xl[n & 3], rs, sx[(n >> 2) & 3]); };
    auto rows_load = [&](int p) {
#pragma unroll
      for (int j = 0; j < 8; ++j) asm volatile("" : "=v"(pq[j]));
      p_load(M_r[p], pq, lv, rs, S_p, S_p + 4096);
      SB();
    };
    auto rows_late = [&]() {
      u32 t = 0u;
#pragma unroll
      for (int j = 0; j < 8; ++j) t = max3u(t, pq[j][1], pq[j][3]);
      return __builtin_amdgcn_ballot_w64(t == ~0u);
    };
    auto rows_sum = [&](int p, auto Y_) {
      if (M_r[p] != 0ull) {
        p_wait<decltype(Y_)::v>(pq);
        unsigned long long late = rows_late();
        if (__builtin_expect(late != 0ull, 0)) {
          int tr = 0;
#pragma unroll 1
          do { asm volatile("s_sleep 1"); p_load(late, pq, lv, rs, S_p, S_p + 4096); p_wait<0>(pq); late = rows_late(); } while (late != 0ull && ++tr < TRIES);
        }
        auto f = [&](int j) { return __builtin_bit_cast(f32x4, pq[j]); };
        const f32x4 v = ((f(0) + f(1)) + (f(2) + f(3))) + ((f(4) + f(5)) + (f(6) + f(7)));
        const auto a = __builtin_amdgcn_permlane32_swap(as_u(v[0]), as_u(v[2]), false, false);
        const auto c = __builtin_amdgcn_permlane32_swap(as_u(v[1]), as_u(v[3]), false, false);
        const auto d = __builtin_amdgcn_permlane16_swap(as_u(as_f(a[0]) + as_f(a[1])), as_u(as_f(c[0]) + as_f(c[1])), false, false);
        const u32 t = as_u(as_f(d[0]) + as_f(d[1]));
        const auto e = __builtin_amdgcn_permlane16_swap(t, t, false, false);
        const u32 st = no_fill(ln_stat(as_f(e[0]), as_f(e[1])));
        const auto r = __builtin_amdgcn_permlane32_swap(st, st, false, false);
        const int sa = (lane & 15) * 8 + (lane >> 4) * 8192;
#pragma unroll
        for (int j = 0; j < S2_R / 4; ++j) __builtin_amdgcn_raw_buffer_store_b64(u32x2{r[0], r[1]}, rs, sa, S_s2 + j * 32768, 16);
        if (p && TL_R1 == 0) vm_wait<S2_R / 4>();
      } else if (p && TL_R1 == 0) vm_wait<0>();
      SB();
    };
    float nr[4], nv[4][2];
    f32x4 ya[2], yc[2];
    f32x2 kk;
    u32 xo[4];
    int ea;
    auto epi_read = [&](int p, int n) {
      const char* yp = sm + q0[3] + p * 4096;
      if (n == 0) { ya[0] = *(const f32x4*)yp; ya[1] = *(const f32x4*)(yp + 8192); }
      if (n == 1) { yc[0] = *(const f32x4*)(yp + 16384); yc[1] = *(const f32x4*)(yp + 24576); }
      if (n == 2) { kk = *(const f32x2*)(sm + LD_KS); ea = *(const int*)(sm + LD_Q + 3 * 4096 + 12 + tid * 16); }
      SB();
    };
    auto epi = [&](int n) {
      if (n < 20) {
        const int k = n & 3, st = n >> 2;
        float &a = nv[k][0], &c = nv[k][1];
        if (st == 0) { a = ya[0][k] + ya[1][k]; pin(a); c = yc[0][k] + yc[1][k]; }
        if (st == 1 || st == 3) { const u32 r = pk_bf(a, c); a = bf_lo(r); c = bf_hi(r); }
        if (st == 2) { a = __builtin_fmaf(a, kk[0], bf_lo(z[0][k])); pin(a); c = __builtin_fmaf(c, kk[1], bf_hi(z[0][k])); }
        if (st == 4) { a += xu[0][k][0]; pin(a); c += xu[0][k][1]; }
        pin(a); pin(c);
      }
      if (n == 20 || n == 21) {
        const int k = 2 * n - 40;
        xo[k] = pk_bf(nv[k][0], nv[k][1]); xo[k + 1] = pk_bf(nv[k + 1][0], nv[k + 1][1]); xo[k] = no_fill(xo[k]); pinu(xo[k]); pinu(xo[k + 1]);
        if (fin && n == 20) { ea += WS_X3 - WS_X1; pinv(ea); }
      }
      if (n == 22) bst<16>(u32x4{xo[0], xo[1], xo[2], xo[3]}, rs, ea);
      SB();
    };
    u32x4 q1t;
    int xdst[2][2], xrd[2], xrq[2];
    auto write_x = [&](int n, int h) {
      char* p = sm + xdst[ff_slot(n) >> 4][h] + (ff_slot(n) & 15) * 4096;
      *(u32*)p = xr[n][2 * h]; *(u32*)(p + 1024) = xr[n][2 * h + 1];
    };
    u32 xm;
    auto check_x = [&](auto G_, auto J_, auto E_) {
      constexpr int g = decltype(G_)::v, j = decltype(J_)::v, c = P.cum[g], n = P.cum[g + 1] - c;
      if constexpr (j == 0) { xm = max3u(xr[c][0], xr[c][2], xr[c + 1][0]); useu(xm); }
      else if constexpr (j < n - 1) { xm = max3u(xm, xr[c + j][2], xr[c + j + 1][0]); useu(xm); }
      else if constexpr (j == n - 1) { xm = max(xm, xr[c + j][2]); useu(xm); }
      else if constexpr (j == n) asm volatile("v_cmp_eq_u32 vcc, -1, %0" :: "v"(xm) : "vcc");
      else {
        cold_begin<64 * fin + g>();
        sfor<c, decltype(E_)::v>([&](auto N_) { constexpr int k = decltype(N_)::v; cold_load<(k >> 4) * 256>(xr[k], xl[k & 3], rs, sx[(k >> 2) & 3]); });
        cold_end_x<64 * fin + g, (XC_W >> (g ? 1 + fin : 0)) & 1 ? decltype(E_)::v - c - n : 0>(S_tr, xm, xr[c], xr[c + 1], xr[c + n - 2 + (n < 4)], xr[c + n - 1]);
      }
      SB();
    };
    int lvw[3], sw[4];
    auto load_wf = [&](auto N_) { constexpr int n = decltype(N_)::v; a_load<((n / 3) & 3) * 1024>(wf[n % 3][n / 3], lvw[n % 3], rs, sw[(n / 3) >> 2]); };
    {
      auto norm = [&](int n) {
        const int k = n & 3, st = n >> 2;
        float &a = nv[k][0], &c = nv[k][1];
        const u32 xk = fin ? xv[k] : gl[k >> 1][1 + 2 * (k & 1)], sk = fin ? sv[k] : gl[2 + (k >> 1)][1 + 2 * (k & 1)];
        if (n < 24) {
          if (st == 0) { nr[k] = __builtin_amdgcn_rcpf(bf_hi(sk)); xu[1][k][0] = bf_lo(xk); xu[1][k][1] = bf_hi(xk); pin(nr[k]); pin(xu[1][k][0]); pin(xu[1][k][1]); }
          if (st == 1) { const float mv = bf_lo(sk); a = xu[1][k][0] - mv; pin(a); c = xu[1][k][1] - mv; }
          if (st == 2 || st == 4) { const u32 r = pk_bf(a, c); a = bf_lo(r); c = bf_hi(r); }
          if (st == 3) { a *= nr[k]; pin(a); c *= nr[k]; }
          if (st == 5) { a *= ds[0]; pin(a); c *= ds[1]; pin(c); z[1][k] = pk_bf(a, c); pinu(z[1][k]); }
          if (st >= 1 && st < 5) { pin(a); pin(c); }
        } else {
          char* up = sm + CV_U + 512 + 2048 + tid * 8;
          *(u32x2*)up = u32x2{pk_h_rtz(nv[0][0], nv[1][0]), pk_h_rtz(nv[2][0], nv[3][0])};
          *(u32x2*)(up + CV_UP) = u32x2{pk_h_rtz(nv[0][1], nv[1][1]), pk_h_rtz(nv[2][1], nv[3][1])};
        }
        SB();
      };
      CSTAMP(9);
      LDS_BARRIER();
      SB();
      CSTAMP(10); TSTAMP(8);
      int la, lb, lsx, lss;
      u32x4 af[4];
      toeplitz(sm, q0, An, [&](auto M_) {
        constexpr int m = decltype(M_)::v, n = tz_wl(fin, m - 1), nx = tz_xl(fin, m - 1);
        if constexpr (!fin && m == TZ_A1 + 6) {
          cold_begin<250>();
          sfor<0, 4>([&](auto N_) { cold_load<decltype(N_)::v * 1024>(af[decltype(N_)::v], lvw[0], rs, lss); });
          cold_end_f<250>(S_tr, xm, af, tag);
          SB();
        }
        if constexpr (m == 0) {
          lvw[0] = lv; lvw[1] = lv + 16384; lvw[2] = w == 3 ? ZT3 : lv + 32768;
          pinv(lvw[0]); pinv(lvw[1]); pinv(lvw[2]);
        }
        if constexpr (m == 1) {
#pragma unroll
          for (int q = 0; q < 4; ++q) { sw[q] = S_wg + q * 4096; pins(sw[q]); }
        }
        if constexpr (!fin && FL_AT == 7 && m == TZ_FL) {
          vm_wait<0>(fla);
          __builtin_amdgcn_raw_buffer_store_b32(tag, rs, fla, 0, 16);
          SB();
        }
        if constexpr (!fin && m == TZ_F0) asm volatile("buffer_load_dword %0, %1, %2, %3 offen sc1" : "=v"(wfl) : "v"(lane * 4), "s"(rs), "s"(S_fl));
        if constexpr (m == tz_wat(fin, 0) - 1) { TSTAMP(9); if constexpr (!fin) w_ready(wfl, tag, lane * 4, rs, S_fl); }
        if constexpr (!fin && FI_AT == 4 && m == tz_wat(fin, 0) - 1) {
          __builtin_amdgcn_raw_buffer_store_b32(tag, rs, lane == 0 ? WS_F2 + b * 16 + w * 4 : 0x7ffffff0, 0, 16);
          SB();
        }
        if constexpr (tz_wl(fin, m) > n) { load_wf(IC<n>{}); SB(); }
        if constexpr (fin && TL_R1 == 2 && m == TZ_P0) rows_load(1);
        if constexpr (fin && TL_R1 == 2 && m == TZ_P1) rows_sum(1, IC<tz_wl(1, TZ_P1) - tz_wl(1, TZ_P0)>{});
        if constexpr (fin && TL_R1 == 3 && m == TZ_P1) rows_sum(1, IC<tz_wl(1, TZ_P1)>{});
        if constexpr (m == TZ_P1) { TSTAMP(10); }
        if constexpr (m == TZ_B2 - 2) epi_read(0, 2);
        if constexpr (m == TZ_B2) { TSTAMP(11); LDS_BARRIER(); SB(); }
        if constexpr (m == 64) { TSTAMP(12); }
        if constexpr (m >= TZ_B2 && m < TZ_B2 + 2) epi_read(0, m - TZ_B2);
        if constexpr (m >= TZ_B2 + 4 && m < TZ_B2 + 4 + TZ_EN) {
          constexpr int e = m - TZ_B2 - 4;
          sfor<(23 * e) / TZ_EN, (23 * (e + 1)) / TZ_EN>([&](auto N_) { epi(decltype(N_)::v); });
        }
        if constexpr (fin && m == TZ_L0 - 1) {
          la = tid * 16; lss = S_sr + 4096; lsx = S_x2 + 4096;
          pinv(la); pins(lss); pins(lsx);
          SB();
        }
        if constexpr (fin && m == TZ_L0) { x_load<0>(sv, la, rs, lss); x_load<0>(xv, la, rs, lsx); SB(); }
        if constexpr (m == TZ_L1) {
          TSTAMP(13);
          if constexpr (fin) {
            x_wait<tz_wl(fin, TZ_L1) - tz_wl(fin, TZ_L0) + (TZ_L0 < TZ_B2 + 3 + TZ_EN)>(sv, xv);
            unsigned long long late = absent(sv, xv);
            if (__builtin_expect(late != 0ull, 0)) {
              int tr = 0;
#pragma unroll 1
              do { re_load(late, sv, xv, la, rs, lss, lsx); late = absent(sv, xv); } while (late != 0ull && ++tr < TRIES);
            }
          }
          SB();
          CSTAMP(11); TSTAMP(14);
        }
        if constexpr (fin && m >= TZ_L1 && m <= TZ_B3) {
          constexpr int d = TZ_B3 + 1 - TZ_L1, e = m - TZ_L1;
          sfor<(21 * e) / d, (21 * (e + 1)) / d>([&](auto N_) { norm(decltype(N_)::v < 20 ? decltype(N_)::v : 24); });
        }
        if constexpr (fin && m > TZ_B3 + 6 && m <= TZ_B3 + 10) norm(m - TZ_B3 + 13);
        if constexpr (!fin && m >= TZ_N0 && m < TZ_B2 - 4) {
          constexpr int d = TZ_B2 - 4 - TZ_N0, e = m - TZ_N0;
          sfor<(25 * e) / d, (25 * (e + 1)) / d>([&](auto N_) { constexpr int k = decltype(N_)::v; norm(k < 20 ? k : k == 20 ? 24 : k - 1); });
        }
        if constexpr (!fin && W2_AT == 4 && m == TZ_V0 - 1) { la = tid * 4 + S_v2; pinv(la); lb = la + 4096; pinv(lb); }
        if constexpr (!fin && W2_AT == 4 && m >= TZ_V0 && m < TZ_V0 + 8 * TZ_VS && (m - TZ_V0) % TZ_VS == 0) {
          constexpr int j = (m - TZ_V0) / TZ_VS, jj = j >> 1, fl = 4 * (jj & 1) + 2 * (j & 1) + (jj >> 1);
          asm volatile("buffer_load_dword %0, %1, %2, 0 offen offset:%3" : "=v"(wv[j]) : "v"(fl < 4 ? la : lb), "s"(r2), "i"((fl & 3) * 1024));
          SB();
        }
        if constexpr (!fin && W2_AT == 4 && m == TZ_E0)
          asm volatile("s_waitcnt vmcnt(%8)" : "+v"(wv[0]), "+v"(wv[1]), "+v"(wv[2]), "+v"(wv[3]), "+v"(wv[4]), "+v"(wv[5]), "+v"(wv[6]), "+v"(wv[7])
                       : "i"(tz_wl(0, TZ_E0) - tz_wl(0, TZ_V0 + 7 * TZ_VS) + (TZ_A0 > TZ_V0 + 7 * TZ_VS && TZ_A0 + 3 < TZ_E0 ? 4 : 0)));
        if constexpr (!fin && m == TZ_E0) put_w2(b, tid);
        if constexpr (!fin && m == TZ_E0 + TZ_ED) { bst<16>(o1, rs, wa + 64); SB(); }
        if constexpr (!fin && m >= TZ_E1 && m < TZ_E1 + NFILL * TZ_ES && (m - TZ_E1) % TZ_ES == 0) fill(b, tid, (m - TZ_E1) / TZ_ES);
        if constexpr (fin && m == TZ_B3) { LDS_BARRIER(); SB(); }
        if constexpr (m == TZ_B3) { TSTAMP(15); }
        constexpr int X0 = tz_xat(fin, 0), C0 = fin ? TZ_C0B : TZ_C0A;
        if constexpr (m == X0 - 10) q1t = *(const u32x4*)(sm + LD_Q + 4096 + tid * 16);
        if constexpr (m >= X0 - 8 && m < X0 - 4) { sx[m - X0 + 8] = S_x + xbuf + (m - X0 + 8) * 524288; pins(sx[m - X0 + 8]); }
        if constexpr (m >= X0 - 3 && m < X0) { xl[m - X0 + 4] = q1t[0] + (m - X0 + 4) * 131072; pinv(xl[m - X0 + 4]); xl[0] = q1t[0]; }
        if constexpr (tz_xl(fin, m) > nx) load_x(IC<nx>{});
        if constexpr (m == C0 - 3) { xdst[0][0] = q1t[1]; xdst[0][1] = q1t[1] + 128; pinv(xdst[0][1]); }
        if constexpr (m == C0 - 2) { xrd[0] = q1t[2]; xrq[0] = q1t[2] + w * 1024; pinv(xrq[0]); }
        if constexpr (m == C0 - 1) vm_wait<tz_xl(fin, C0 - 1) - 4>();
        if constexpr (m >= C0 && m < C0 + 6) check_x(IC<0>{}, IC<m - C0>{}, IC<tz_xl(fin, m)>{});
        if constexpr (m >= C0 + 6) {
          constexpr int d = 144 - C0 - 6, e = m - C0 - 6;
          sfor<(8 * e + d - 1) / d, (8 * (e + 1) + d - 1) / d>([&](auto N_) { write_x(decltype(N_)::v >> 1, decltype(N_)::v & 1); });
        }
        if constexpr (!fin && m == TZ_A0 - 1) { lss = FI_AT == 4 ? WS_F2 : WS_FL; pins(lss); }
        if constexpr (!fin && m >= TZ_A0 && m < TZ_A0 + 4) x_load<(m - TZ_A0) * 1024>(af[m - TZ_A0], lvw[0], rs, lss);
        if constexpr (!fin && m == TZ_A1)
          asm volatile("s_waitcnt vmcnt(%4)" : "+v"(af[0]), "+v"(af[1]), "+v"(af[2]), "+v"(af[3]) : "i"(tz_wl(0, TZ_A1) - tz_wl(0, TZ_A0 + 3)));
        if constexpr (!fin && m > TZ_A1 && m <= TZ_A1 + 4) {
          constexpr int j = m - TZ_A1 - 1;
          const u32 t = __builtin_amdgcn_bitop3_b32(af[j][0], af[j][1], tag, 0x7e) | __builtin_amdgcn_bitop3_b32(af[j][2], af[j][3], tag, 0x7e);
          xm = j ? xm | t : t; useu(xm);
        }
        SB();
        if constexpr (!fin && m == TZ_A1 + 5) asm volatile("v_cmp_ne_u32 vcc, 0, %0" :: "v"(xm) : "vcc");
        SB();
      });
      static_assert(tz_wat(fin, NW - 1) < tz_xat(fin, 0) && tz_xl(fin, 143) == P.n0 && TZ_B2 + 4 + TZ_EN <= TZ_L1 && TZ_L1 < tz_xat(1, 0) &&
                    TZ_E0 + TZ_ED < tz_xat(0, 0) && TZ_E1 + (NFILL - 1) * TZ_ES < tz_xat(0, 0) && tz_wat(fin, 0) >= 3 && P.cum[1] == 4 && TZ_A1 + 6 <= tz_xat(0, 0) && TZ_A0 + 3 < TZ_A1 &&
                    (FF_SV < 0 || (FL_AT == 2 && W2_AT != 3 && E_AT != 3)) && (FL_AT != 7 || (POLL_AT == 1 && TZ_FL < TZ_F0 && TZ_F0 < TZ_W0A - 1 && TZ_FL < TZ_B2)), "");
      CSTAMP(12);
      CSTAMP(13);
    }
    int s2[11], sh[12], lvd[4];
    u32x4 hf[22][2];
    FSTAMP(16);
    u32x4 ones = {fresh<0x3f803f80u>(), fresh<0x3f803f80u>(), fresh<0x3f803f80u>(), fresh<0x3f803f80u>()};
    auto read_xf = [&](int c, int i) { return *(const u32x4*)(sm + xrd[ff_slot(c) >> 4] + (ff_slot(c) & 15) * 4096 + i * 1024); };
    auto read_xq = [&](int c) { return *(const u32x4*)(sm + xrq[ff_slot(c) >> 4] + (ff_slot(c) & 15) * 4096); };
    WG_BARRIER();
    FSTAMP(17);
    f32x4 acc[2][3][4], q1[2], q2[2];
    u32x4 xf[2][4], xq[2];
    epi_read(1, 0); epi_read(1, 1);
    xq[0] = read_xq(0); xq[1] = read_xq(1);
#pragma unroll
    for (int i = 0; i < 4; ++i) xf[0][i] = read_xf(0, i);
    SB();
    {
      f32x4 a = ya[0] + ya[1], c = yc[0] + yc[1];
      asm volatile("" : "+v"(a), "+v"(c));
#pragma unroll
      for (int k = 0; k < 4; ++k) {
        u32 r = pk_bf(a[k], c[k]);
        const f32x2 y = __builtin_elementwise_fma(f32x2{bf_lo(r), bf_hi(r)}, kk, f32x2{bf_lo(z[1][k]), bf_hi(z[1][k])});
        r = pk_bf(y[0], y[1]);
        const f32x2 v = f32x2{bf_lo(r), bf_hi(r)} + f32x2{xu[1][k][0], xu[1][k][1]};
        xo[k] = pk_bf(v[0], v[1]);
      }
      bst<16>(u32x4{no_fill(xo[0]), xo[1], no_fill(xo[2]), xo[3]}, rs, ea + 256);
      SB();
    }
    u32x4 q2t, q3t;
    int ra = q1t[3], tra, sva;
    u32x2 res[4];
    auto prep = [&](int n) {
      if (n == 0) q2t = *(const u32x4*)(sm + LD_Q + 8192 + tid * 16);
      else if (n < 3) { xdst[1][n - 1] = xdst[0][n - 1] + 65536; pinv(xdst[1][n - 1]); }
      else if (n == 3) { xrd[1] = xrd[0] + 65536; pinv(xrd[1]); }
      else if (n == 4) { xrq[1] = xrq[0] + 65536; pinv(xrq[1]); }
      else if (n == 5) { lvd[0] = tid * 16; pinv(lvd[0]); }
      else if (n < 9) { lvd[n - 5] = lvd[0] ^ ((n - 5) * 1024); pinv(lvd[n - 5]); }
      else if (n == 9) { tra = (lvd[0] & 0xf0) | FF_T; pinv(tra); }
      else if (n == 10) { sva = (tid & 0xf0) | LD_SV; pinv(sva); }
      else if (n < 22) { s2[n - 11] = S_w2 + (n - 11) * 4096; pins(s2[n - 11]); }
      else if (n < 34) { sh[n - 22] = S_h + (((n - 22) / 6) * 88 + ((n - 22) % 6) * 4) * 1024; pins(sh[n - 22]); }
      else { ra += S_rd; pinv(ra); }
    };
    float ta, tb, tc;
    u32 tu;
    f32x4 rc[2][3];
    auto stat = [&](int p, int st) {
      if (st == 0) asm volatile("v_cndmask_b32 %0, %1, %2, %3" : "=v"(ta) : "v"(q2[p][0]), "v"(q2[p][1]), "s"(M_1));
      if (st == 1) asm volatile("v_cndmask_b32 %0, %1, %2, %3" : "=v"(tb) : "v"(q2[p][2]), "v"(q2[p][3]), "s"(M_1));
      if (st == 2) { asm volatile("" :: "v"(q1[p])); tc = q1[p][0] * (-1.f / D_); pin(tc); }
      if (st == 3) asm volatile("v_cndmask_b32 %0, %0, %1, %2" : "+v"(ta) : "v"(tb), "s"(M_2));
      if (st == 4) { tb = tc * tc; pin(tb); }
      if (st == 5) { ta = __builtin_fmaf(ta, 1.f / D_, -tb); pin(ta); }
      if (st == 6) { ta = fmaxf(ta, 0.f); pin(ta); }
      if (st == 7) { tu = pk_bf(ta, tc); pinu(tu); }
      if (st == 8) { ta = bf_lo(tu); pin(ta); }
      if (st == 9) { tc = bf_hi(tu); pin(tc); }
      if (st == 10) { ta += 1e-5f; pin(ta); }
      if (st == 11) *(float*)(sm + q2t[3] + p * 768) = tc;
      if (st == 12 || st == 15) { tu = pk_bf(ta, ta); pinu(tu); }
      if (st == 13 || st == 16) { ta = bf_lo(tu); pin(ta); }
      if (st == 14) { ta = __builtin_amdgcn_sqrtf(ta); pin(ta); }
      if (st == 17) { tb = -1.44269504089f * ta; pin(tb); }
      if (st == 18) { ta = __builtin_amdgcn_rcpf(ta); pin(ta); }
      if (st == 19) *(float*)(sm + q2t[3] + p * 768 + 512) = tb;
      if (st == 20) { ta *= -1.44269504089f; pin(ta); }
      if (st == 21) *(float*)(sm + q2t[3] + p * 768 + 256) = ta;
    };
    float z0, z1, e0, e1, u0, u1;
    u32 o[4];
    auto mark = [&](u32 v) { return fin ? v | 1u : v & ~1u; };
    auto uop = [&](int u) {
      const int tl = u >> 7, i = (u >> 5) & 3, st = u & 31;
      const f32x4 &nm = rc[0][0], &c1 = rc[0][1], &ic = rc[0][2];
      if (st >= 17) {
        if (st < 21) {
          const int a = st == 18 ? 1 : st == 20 ? 2 : 0, b = st == 19 ? 1 : st == 17 ? 2 : 3;
          if (st < 19) { const auto r = __builtin_amdgcn_permlane32_swap(o[a], o[b], false, false); o[a] = r[0]; o[b] = r[1]; }
          else { const auto r = __builtin_amdgcn_permlane16_swap(o[a], o[b], false, false); o[a] = r[0]; o[b] = r[1]; }
          pinu(o[a]); pinu(o[b]);
        } else if (st < 23) { o[2 * st - 42] = mark(o[2 * st - 42]); pinu(o[2 * st - 42]); }
        else bst<16>(u32x4{o[0], o[1], o[2], o[3]}, rs, q2t[tl]);
        return;
      }
      if (st == 0) { z0 = __builtin_fmaf(nm[i], Sv[tl][0], acc[0][tl][i][0]); pin(z0); }
      if (st == 1) { z1 = __builtin_fmaf(nm[i], Sv[tl][1], acc[0][tl][i][1]); pin(z1); }
      if (st == 2) { z0 *= c1[i]; pin(z0); }
      if (st == 3) { z1 *= c1[i]; pin(z1); }
      if (st == 4) { e0 = __builtin_amdgcn_exp2f(z0); pin(e0); }
      if (st == 5) { e1 = __builtin_amdgcn_exp2f(z1); pin(e1); }
      if (st == 6) { u0 = __builtin_fmaf(nm[i], Sv[tl][2], acc[0][tl][i][2]); pin(u0); }
      if (st == 7) { u1 = __builtin_fmaf(nm[i], Sv[tl][3], acc[0][tl][i][3]); pin(u1); }
      if (st == 8) { e0 = __builtin_fmaf(e0, ic[i], ic[i]); pin(e0); }
      if (st == 9) { e1 = __builtin_fmaf(e1, ic[i], ic[i]); pin(e1); }
      if (st == 10) { e0 = __builtin_amdgcn_rcpf(e0); pin(e0); }
      if (st == 11) { e1 = __builtin_amdgcn_rcpf(e1); pin(e1); }
      if (st == 12) { u0 *= z0; pin(u0); }
      if (st == 13) { u1 *= z1; pin(u1); }
      if (st == 14) { u0 *= e0; pin(u0); }
      if (st == 15) { u1 *= e1; pin(u1); }
      if (st == 16) { o[i] = pk_bf(u0, u1); pinu(o[i]); }
    };
    f32x2 pz[2][2], pe[2][2], pu[2][2];
    auto epi2 = [&](int c, int st) {
      if (c < 0 || c > 5) return;
      const int tl = c >> 1, ih = c & 1;
      const f32x4 &nm = rc[1][0], &c1 = rc[1][1], &ic = rc[1][2];
      f32x2 *z = pz[c & 1], *e = pe[c & 1], *u = pu[c & 1];
#pragma unroll
      for (int q = 0; q < 2; ++q) {
        const int i = 2 * ih + q;
        if (st == 0) {
          z[q] = __builtin_elementwise_fma(f32x2{nm[i], nm[i]}, f32x2{Sv[tl][0], Sv[tl][1]}, f32x2{acc[1][tl][i][0], acc[1][tl][i][1]});
          u[q] = __builtin_elementwise_fma(f32x2{nm[i], nm[i]}, f32x2{Sv[tl][2], Sv[tl][3]}, f32x2{acc[1][tl][i][2], acc[1][tl][i][3]});
          z[q] *= f32x2{c1[i], c1[i]};
        }
        if (st == 1) { e[q][0] = __builtin_amdgcn_exp2f(z[q][0]); e[q][1] = __builtin_amdgcn_exp2f(z[q][1]); u[q] *= z[q]; }
        if (st == 2) e[q] = __builtin_elementwise_fma(e[q], f32x2{ic[i], ic[i]}, f32x2{ic[i], ic[i]});
        if (st == 3) { e[q][0] = __builtin_amdgcn_rcpf(e[q][0]); e[q][1] = __builtin_amdgcn_rcpf(e[q][1]); }
        if (st == 4) { u[q] *= e[q]; o[i] = pk_bf(u[q][0], u[q][1]); }
      }
      if (st == 5 && ih) {
        const auto a02 = __builtin_amdgcn_permlane32_swap(o[0], o[2], false, false);
        const auto a13 = __builtin_amdgcn_permlane32_swap(o[1], o[3], false, false);
        const auto lo = __builtin_amdgcn_permlane16_swap(a02[0], a13[0], false, false);
        const auto hi = __builtin_amdgcn_permlane16_swap(a02[1], a13[1], false, false);
        bst<16>(u32x4{mark(lo[0]), lo[1], mark(hi[0]), hi[1]}, rs, q2t[tl] + 88 * 1024);
      }
      SB();
    };
    f32x4 ac[4][4], rd[6], q3[4];
    u32x2 ov[2], one2;
    float ru[8], fu[8];
    auto oprep = [&](int n) {
      if (n == 0) q3t = *(const u32x4*)(sm + LD_Q + 12288 + tid * 16);
      else if (n < 9) { const u32 r = res[(n - 1) >> 2][((n - 1) >> 1) & 1]; ru[n - 1] = (n & 1) ? bf_lo(r) : bf_hi(r); pin(ru[n - 1]); }
      else if (!fin) one2[n - 9] = fresh<0x3f803f80u>();
    };
    auto outw = [&](int p, int n) { *(f32x4*)(sm + lvd[1 + (n >> 1)] + FF_RS + n * 4096) = ac[2 * p + (n & 1)][1 + (n >> 1)]; };
    auto outr = [&](int n) { rd[n] = *(const f32x4*)(sm + lvd[0] + FF_RS + n * 4096); };
    auto out_end = [&](int p, int n) {
      if constexpr (fin) {
        if (n < 2) {
          const auto x = __builtin_amdgcn_permlane16_swap(ov[0][n], ov[1][n], false, false);
          u32 a = x[0], c = x[1];
          pinu(a); pinu(c);
          ov[0][n] = a; ov[1][n] = c;
        }
        const rsrc_t ro = __builtin_amdgcn_make_buffer_rsrc(outp + p * (MB / 2), (short)0, MB, 0x00020000);
        if (n == 2) __builtin_amdgcn_raw_buffer_store_b128(u32x4{ov[0][0], ov[0][1], ov[1][0], ov[1][1]}, ro, q3t[2], 0, OUT_AUX);
      } else {
        if (n < 2) { u32 a = no_fill(ov[0][n]); pinu(a); ov[0][n] = a; }
        if (n == 2 || n == 3) mma_s(q3[3 - n], ov[3 - n], ov[3 - n]);
        if (n == 4 || n == 5) mma_s(q3[7 - n], one2, ov[5 - n]);
        if (n == 6) { u32 a = ov[0][1], c = ov[1][0]; asm volatile("v_swap_b32 %0, %1" : "+v"(a), "+v"(c)); ov[0][1] = a; ov[1][0] = c; }
        if (n == 6 || n == 7) __builtin_amdgcn_raw_buffer_store_b64(ov[n - 6], rs, q3t[1], p * 4096 + (n - 6) * 8192, 16);
        if (n == 14) asm volatile("" : "+v"(q3[2]));
        if (n >= 8 && n < 12) asm volatile("v_cndmask_b32 %0, %1, %2, %3" : "=v"(fu[n - 8]) : "v"(q3[1 - ((n - 8) >> 1)][2 * (n & 1)]), "v"(q3[1 - ((n - 8) >> 1)][2 * (n & 1) + 1]), "s"(M_1));
        if (n == 12 || n == 13) asm volatile("v_cndmask_b32 %0, %0, %1, %2" : "+v"(fu[2 * n - 24]) : "v"(fu[2 * n - 23]), "s"(M_2));
        if (n == 14) { float a = q3[3][0]; asm volatile("v_mov_b32 %0, %1" : "=v"(a) : "v"(q3[3][0]), "v"(q3[3])); q3[2][2] = a; }
        if (n == 15 || n == 16) { u32 d = no_fill(as_u(fu[2 * n - 30])); pinu(d); q3[2][33 - 2 * n] = as_f(d); }
        if (n == 17) __builtin_amdgcn_raw_buffer_store_b128(__builtin_bit_cast(u32x4, q3[2]), rs, q3t[0], p * 262144, 16);
      }
    };
    auto outs = [&](int n) {
      if (n < 24) { float a = ac[(n >> 2) & 1][0][n & 3] + rd[n >> 2][n & 3]; pin(a); ac[(n >> 2) & 1][0][n & 3] = a; }
      else if (n < 28) { u32 a = pk_bf(ac[(n >> 1) & 1][0][2 * (n & 1)], ac[(n >> 1) & 1][0][2 * (n & 1) + 1]); pinu(a); ov[(n >> 1) & 1][n & 1] = a; }
      else if (n < 36) { const u32 f = ov[(n - 28) >> 2][(n >> 1) & 1]; fu[n - 28] = (n & 1) ? bf_hi(f) : bf_lo(f); pin(fu[n - 28]); }
      else if (n < 44) { fu[n - 36] += ru[n - 36]; pin(fu[n - 36]); }
      else if (n < 48) { u32 a = pk_bf(fu[2 * n - 88], fu[2 * n - 87]); pinu(a); ov[(n >> 1) & 1][n & 1] = a; }
      else if (n < n_out(fin) - 4) out_end(0, n - 48);
      else { lvd[n - n_out(fin) + 4] += 24576; pinv(lvd[n - n_out(fin) + 4]); }
    };
    auto check_h = [&](auto K_, auto J_) {
      constexpr int k = decltype(K_)::v, j = decltype(J_)::v;
      if constexpr (j == 0) { xm = __builtin_amdgcn_bitop3_b32(hf[k][0][0], hf[k][0][2], hf[k][1][0], fin ? 0x80 : 0xfe); useu(xm); }
      if constexpr (j == 1) { xm = __builtin_amdgcn_bitop3_b32(xm, hf[k][1][2], 1u, fin ? 0x80 : 0xa8); useu(xm); }
      if constexpr (j == 2) asm volatile("v_cmp_ne_u32 vcc, %1, %0" :: "v"(xm), "i"(fin) : "vcc");
      if constexpr (j == 3) {
        cold_begin<64 * fin + 16 + k>();
        sfor<2 * k, P.hi[k]>([&](auto N_) { constexpr int n = decltype(N_)::v; cold_load<((n % 22) & 3) * 1024>(hf[n >> 1][n & 1], lv, rs, sh[(n / 22) * 6 + ((n % 22) >> 2)]); });
        cold_end_h<64 * fin + 16 + k, fin, HC_W ? P.hi[k] - 2 * k - 2 : 0>(S_tr, xm, hf[k][0], hf[k][1]);
      }
    };
    u32 touch2;
    u32x4 af2;
    auto act = [&](auto U_) {
      constexpr int t = P.code[decltype(U_)::v] >> 10, a = P.code[decltype(U_)::v] & 1023;
      if constexpr (t == A_XF) xf[(a >> 2) & 1][a & 3] = read_xf(a >> 2, a & 3);
      if constexpr (t == A_XQ) xq[a & 1] = read_xq(a);
      if constexpr (t == A_XW) write_x(a >> 1, a & 1);
      if constexpr (t == A_XL) load_x(IC<a>{});
      if constexpr (t == A_CK) check_x(IC<(a >> 3)>{}, IC<(a & 7)>{}, IC<P.xi[a >> 3]>{});
      if constexpr (t == A_WL) load_wf(IC<a>{});
      if constexpr (t == A_SV) v_load<a * 1024>(Sv[a], lv, rs, S_sv);
      if constexpr (t == A_W2) a_load<0>(w2[a >> 2][a & 3], lvd[a & 3], rs, s2[a >> 2]);
      if constexpr (t == A_H) x_load<((a % 22) & 3) * 1024>(hf[a >> 1][a & 1], lv, rs, sh[(a / 22) * 6 + ((a % 22) >> 2)]);
      if constexpr (t == A_FI) {
        if (a == 0) { f16 = u32x4{fresh<~0u>(), fresh<~0u>(), fresh<~0u>(), fresh<~0u>()}; ea += WS_X3 - WS_X1; pinv(ea); }
        if (a < 2) bst<16>(f16, rs, ea + a * 256);
        else { int fa = lv + (a < 4 ? S_f1 : S_f2); pinv(fa); bst<16>(f16, rs, fa + (a == 3 ? 1024 : 0)); }
      }
      if constexpr (t == A_F2) { u32 tv = tag; pinu(tv); __builtin_amdgcn_raw_buffer_store_b32(tv, rs, 0, S_f3, 16); }
      if constexpr (t == A_AL) { S_f3 = WS_F2 + w * 1024; pins(S_f3); x_load<0>(af2, lv, rs, S_f3); }
      if constexpr (t == A_AC) {
        if constexpr (a == 0) { xm = __builtin_amdgcn_bitop3_b32(af2[0], af2[1], tag, 0x7e); useu(xm); }
        if constexpr (a == 1 || a == 2) { xm = __builtin_amdgcn_bitop3_b32(xm, af2[a + 1], tag, 0xf6); useu(xm); }
        if constexpr (a == 3) asm volatile("v_cmp_ne_u32 vcc, 0, %0" :: "v"(xm) : "vcc");
        if constexpr (a == 4) {
          cold_begin<251>();
          cold_load<0>(af2, lv, rs, S_f3);
          cold_end_g<251>(S_tr, xm, af2, tag);
        }
      }
      if constexpr (t == A_VW) vm_wait<a>();
      if constexpr (t == A_ST) stat(a >> 5, a & 31);
      if constexpr (t == A_TR) rc[a >> 2][a & 3] = *(const f32x4*)(sm + tra + (a >> 2) * 768 + (a & 3) * 256);
      if constexpr (t == A_RS) res[a] = *(const u32x2*)(sm + ra + (a & 1) * 1024);
      if constexpr (t == A_PR) prep(a);
      if constexpr (t == A_SR) {
        float v = Sv[(a % 12) >> 2][a & 3];
        asm volatile("v_add_f32_dpp %0, %0, %0 row_ror:%1 row_mask:0xf bank_mask:0xf" : "+v"(v) : "i"(8 >> (a / 12)));
        Sv[(a % 12) >> 2][a & 3] = v;
      }
      if constexpr (t == A_SS && a < 4) *(f32x4*)(sm + sva + a * 256) = Sv[a];
      if constexpr (t == A_SS && a >= 4) Sv[a - 4] = *(const f32x4*)(sm + sva + (a - 4) * 256);
      if constexpr (t == A_U) uop(a);
      if constexpr (t == A_HC) check_h(IC<(a >> 2)>{}, IC<(a & 3)>{});
      if constexpr (t == A_OW) outw(0, a);
      if constexpr (t == A_OR) outr(a);
      if constexpr (t == A_O) outs(a);
      if constexpr (t == A_RL) rows_load(0);
      if constexpr (t == A_OP) oprep(a);
      if constexpr (t == A_TC) asm volatile("global_load_dword %0, %1, %2" : "=v"(touch2) : "v"(min((lane & 3) * 16384 + (lane & 4) * TC_H + S_ln, CODE_LEN)), "s"(pc));
      SB();
    };
    auto site = [&](auto S_) { sfor<P.first[decltype(S_)::v], P.first[decltype(S_)::v + 1]>(act); };
    sfor<0, 32>([&](auto S_) {
      constexpr int s = decltype(S_)::v, p = s >> 4, ks = s & 15, sc = P.stat[s], o4 = sc >= 0 ? 4 : 0;
      sfor<0, 12 + o4>([&](auto G_) {
        constexpr int gap = decltype(G_)::v, i = (gap - o4) / 3, tl = (gap - o4) % 3;
        if constexpr (gap >= o4) mma_w<ks == 0>(acc[p][tl][i], wf[tl][ks], xf[s & 1][i]);
        else if constexpr (gap & 1) mma_v<(sc & 7) == 0 && gap < 2>(q1[sc >> 3], ones, xq[gap >> 1]);
        else mma_v<(sc & 7) == 0 && gap < 2>(q2[sc >> 3], xq[gap >> 1], xq[gap >> 1]);
        SB();
        site(IC<16 * s + gap>{});
      });
      if constexpr (P.endw[s] < 0) WG_BARRIER();
      else { __builtin_amdgcn_s_waitcnt(0xc07f | (P.endw[s] << 8)); SB(); }
      if constexpr (s == 15) { FSTAMP(18); }
    });
    FSTAMP(19);
    site(IC<S_MID>{}); site(IC<S_MID + 1>{});
    MFMA_DRAIN();
    epi2(0, 0); epi2(0, 1);
    sfor<0, 6>([&](auto E_) {
      constexpr int e = decltype(E_)::v;
      epi2(e, 2); epi2(e + 1, 0); epi2(e, 3); epi2(e + 1, 1); epi2(e, 4);
      site(IC<S_MID + 2 + e>{});
      epi2(e, 5);
    });
    FSTAMP(20);
    site(IC<S_MID + 8>{});
    sfor<0, 22>([&](auto K_) {
      constexpr int k = decltype(K_)::v, p = k / 11, i = k % 11;
      sfor<0, 8>([&](auto N_) {
        constexpr int n = decltype(N_)::v, t = n >> 2, d = n & 3, q = 8 * k + n;
        mma_w<i == 0>(ac[2 * p + t][d], w2[i][d], hf[k][t]);
        SB();
        site(IC<S_DN + q>{});
        if constexpr (q == 88) { DSTAMP(16); }
        if constexpr (q == 96) { DSTAMP(17); }
        if constexpr (q == 110) { DSTAMP(18); }
        if constexpr (q == 123) { DSTAMP(19); }
        if constexpr (q == 145) { DSTAMP(20); }
        if constexpr (q == 175) { DSTAMP(23); }
      });
      if constexpr (k == FF_BQ || k == FF_OW[fin][2]) WG_BARRIER();
      if constexpr (k == 10) { FSTAMP(21); }
    });
    MFMA_DRAIN();
    FSTAMP(22);
    sfor<0, 6>([&](auto N_) { outw(1, decltype(N_)::v); SB(); });
    if constexpr (!fin && TL_R0 == 0) rows_sum(0, IC<0>{});
    WG_BARRIER();
    sfor<0, 6>([&](auto N_) { outr(decltype(N_)::v); SB(); });
    if constexpr (!fin) q0 = *(const u32x4*)(sm + LD_Q + tid * 16);
    int xa = tid * 16;
    if constexpr (!fin) {
      pinv(xa);
      asm volatile("" :: "v"(touch2));
    }
    SB();
#pragma unroll
    for (int t = 0; t < 2; ++t) {
      const f32x4 v = (ac[2 + t][0] + rd[t]) + (rd[2 + t] + rd[4 + t]);
      const u32 f0 = pk_bf(v[0], v[1]), f1 = pk_bf(v[2], v[3]);
      const u32x2 r = res[2 + t];
      ov[t] = u32x2{pk_bf(bf_lo(r[0]) + bf_lo(f0), bf_hi(r[0]) + bf_hi(f0)), pk_bf(bf_lo(r[1]) + bf_lo(f1), bf_hi(r[1]) + bf_hi(f1))};
    }
    if constexpr (fin) {
      sfor<0, 3>([&](auto N_) { out_end(1, decltype(N_)::v); SB(); });
      FSTAMP(23);
      ST_SAVE(4, 8); ST_SAVE(6, 16);
      return;
    }
    asm volatile("" : "+v"(ov[0]), "+v"(ov[1]));
    SB();
    sfor<0, 8>([&](auto N_) { out_end(1, decltype(N_)::v); SB(); });
    asm volatile("s_nop 3");
    sfor<8, 18>([&](auto N_) { out_end(1, decltype(N_)::v); SB(); });
    FSTAMP(23);
    ST_SAVE(1, 8); ST_SAVE(2, 16);
    STAMP(0);
    {
      if (TL_R0 == 1) rows_sum(0, IC<3>{});
      near();
      STAMP(1);
      asm volatile("s_sleep %0" :: "i"(TL_SA));
      if (TL_R1 < 2) rows_load(1);
      asm volatile("s_sleep %0" :: "i"(TL_SB));
      x_load<0>(sv, xa, rs, S_sr); x_load<0>(xv, xa, rs, S_x2);
      SB();
      if (TL_R1 == 0) rows_sum(1, IC<2>{});
      x_wait<TL_R1 ? 0 : S2_R / 4>(sv, xv);
      unsigned long long late = absent(sv, xv);
      if (__builtin_expect(late != 0ull, 0)) {
        int tr = 0;
#pragma unroll 1
        do { re_load(late, sv, xv, xa, rs, S_sr, S_x2); late = absent(sv, xv); } while (late != 0ull && ++tr < TRIES);
      }
      SB();
      CSTAMP(8);
      if (TL_R1 == 3) rows_load(1);
      norm0(tid, {xv[0], xv[1], xv[2], xv[3]}, {sv[0], sv[1], sv[2], sv[3]});
      if (TL_R1 == 1) rows_sum(1, IC<0>{});
      STAMP(2);
      STAMP(3);
      ST_SAVE(3, 0);
    }
  };
  layer(IC<0>{});
  layer(IC<1>{});
  ST_FLUSH();
}

extern "C" void kernel_launch(void* const* d_in, const int* in_sizes, int n_in, void* d_out, int out_size,
                              void* d_ws, size_t ws_size, hipStream_t stream) {
  k_all<<<256, 256, 0, stream>>>((const u16*)d_in[0], (const u16*)d_in[1], (const u16*)d_in[2], (const u16*)d_in[3],
                                 (const u16*)d_in[4], (const u16*)d_in[5], (const u16*)d_in[6], (const u16*)d_in[8],
                                 (const u16*)d_in[9], (const u16*)d_in[10], (u16*)d_out, (char*)d_ws);
}
```
